```python
import torch

def _softmax(x, axis=-1):
    return torch.softmax(x, dim=axis)
def _gelu(x):
    return torch.nn.functional.gelu(x, approximate="tanh")
def _silu(x):
    return torch.nn.functional.silu(x)
def _sigmoid(x):
    return torch.sigmoid(x)
def _layernorm(x, axis=-1, eps=1e-5):
    m = x.mean(dim=axis, keepdim=True)
    v = x.var(dim=axis, keepdim=True, unbiased=False)
    return (x - m) / torch.sqrt(v + eps)
def _scatter_add(tgt, idx, src, axis=0):
    return tgt.clone().index_add_(axis, idx, src)
def _segment_sum(src, seg, n):
    out = torch.zeros((n,) + src.shape[1:], dtype=src.dtype)
    return out.index_add_(0, seg, src)
def _pad(x, pw):
    flat = tuple(v for pair in reversed(pw) for v in pair)
    return torch.nn.functional.pad(x, flat)

kernel_name = 'recurrent_scan__sliding_local_dilated__geglu_ffn__prompt'

def setup_inputs():

    _w = (lambda a: (lambda t: t.to(torch.bfloat16) if t.is_floating_point() else t)(torch.as_tensor(a)))
    def _A_setup():
        B, S, D = 1, 2048, 576
        return {
            "x": _w((lambda *s: torch.randn(*s, dtype=torch.bfloat16) * 0.05)(B, S, D)),
            "wqkv": _w((lambda *s: torch.randn(*s, dtype=torch.bfloat16) * 0.05)(D, 3 * D)),
            "wo": _w((lambda *s: torch.randn(*s, dtype=torch.bfloat16) * 0.05)(D, D)),
        }
    _A_in = _A_setup()

    def _B_setup():
        B, S, D, F = 1, 2048, 576, 4608
        return {
            "x": _w((lambda *s: torch.randn(*s, dtype=torch.bfloat16) * 0.05)(B, S, D)),
            "w_gate": _w((lambda *s: torch.randn(*s, dtype=torch.bfloat16) * 0.05)(D, F)),
            "w_up": _w((lambda *s: torch.randn(*s, dtype=torch.bfloat16) * 0.05)(D, F)),
            "w_down": _w((lambda *s: torch.randn(*s, dtype=torch.bfloat16) * 0.05)(F, D)),
        }
    _B_in = _B_setup()

    return {
        "A_x": _A_in["x"],
        "A_wqkv": _A_in["wqkv"],
        "A_wo": _A_in["wo"],
        "B_x": _B_in["x"],
        "B_w_gate": _B_in["w_gate"],
        "B_w_up": _B_in["w_up"],
        "B_w_down": _B_in["w_down"],
    }

def reference(A_x, A_wqkv, A_wo, B_x, B_w_gate, B_w_up, B_w_down):
    def _A_ref(x, wqkv, wo):
        B, S, D = x.shape
        H = 32; Dh = D // H
        W, dil = 8, 2
        qkv = (lambda x, *d: x.permute(*d))((x @ wqkv).reshape(B, S, 3, H, Dh), 2, 0, 3, 1, 4)
        q, k, v = qkv[0], qkv[1], qkv[2]
        scale = Dh ** -0.5
        pos = torch.arange(S)
        off = torch.arange(W) * dil
        idx = pos[:, None] - off[None, :]
        mask = idx >= 0
        idx_c = torch.where(mask, idx, 0).reshape(-1)
        kg = k[:, :, idx_c, :].reshape(B, H, S, W, Dh)
        vg = v[:, :, idx_c, :].reshape(B, H, S, W, Dh)
        logits = torch.einsum("bhsd,bhswd->bhsw", q, kg) * scale
        logits = torch.where(mask[None, None, :, :], logits, -1e9)
        attn = _softmax(logits, axis=-1)
        o = torch.einsum("bhsw,bhswd->bhsd", attn, vg)
        return (lambda x, *d: x.permute(*d))(o, 0, 2, 1, 3).reshape(B, S, D) @ wo

    def _B_ref(x, w_gate, w_up, w_down):
        return (_gelu(x @ w_gate) * (x @ w_up)) @ w_down

    x = A_x
    xn = _layernorm(x, axis=-1)
    s = _A_ref(xn, A_wqkv, A_wo)
    g = _silu(_B_ref(xn, B_w_gate, B_w_up, B_w_down))
    return x + s * g
```

```cpp
#include <hip/hip_runtime.h>
#include <hip/hip_bf16.h>
typedef __hip_bfloat16 bf16;
constexpr int S=2048, D=576, F=4608, H=32, DH=18, W=8, DIL=2;
__device__ __forceinline__ float b2f(bf16 v){ return __bfloat162float(v);}
__device__ __forceinline__ bf16 f2b(float v){ return __float2bfloat16(v);}
__device__ __forceinline__ float rb(float v){ return __bfloat162float(__float2bfloat16(v));}

__global__ void ln_k(const bf16* x, bf16* xn){
  int row=blockIdx.x; int t=threadIdx.x;
  float v[9]; float s=0;
  for(int i=0;i<9;i++){ v[i]=b2f(x[row*D+t+64*i]); s+=v[i]; }
  for(int o=32;o>0;o>>=1) s+=__shfl_xor(s,o);
  float mf=s/D; float m=rb(mf);
  float q=0; for(int i=0;i<9;i++){ float d=v[i]-mf; q+=d*d; }
  for(int o=32;o>0;o>>=1) q+=__shfl_xor(q,o);
  float var=rb(q/D);
  float den=rb(sqrtf(rb(var+1e-5f)));
  for(int i=0;i<9;i++){ xn[row*D+t+64*i]=f2b(rb(v[i]-m)/den); }
}
__global__ void gemm_k(const bf16* A,const bf16* B,bf16* C,int M,int N,int K){
  __shared__ float As[16][17], Bs[16][17];
  int tx=threadIdx.x, ty=threadIdx.y; int row=blockIdx.y*16+ty, col=blockIdx.x*16+tx;
  float acc=0;
  for(int k0=0;k0<K;k0+=16){
    As[ty][tx]=b2f(A[(long)row*K+k0+tx]); Bs[ty][tx]=b2f(B[(long)(k0+ty)*N+col]);
    __syncthreads();
    for(int k=0;k<16;k++) acc+=As[ty][k]*Bs[k][tx];
    __syncthreads();
  }
  C[(long)row*N+col]=f2b(acc);
}
__global__ void attn_k(const bf16* qkv, bf16* o){
  int s=blockIdx.x; int h=threadIdx.x; if(h>=H) return;
  float q[DH]; for(int d=0;d<DH;d++) q[d]=b2f(qkv[(long)s*3*D+h*DH+d]);
  float lg[W]; float mx=-1e30f; const float scale=rb(0.23570226039551584f);
  for(int w=0;w<W;w++){ int p=s-w*DIL; float a;
    if(p>=0){ a=0; for(int d=0;d<DH;d++) a+=q[d]*b2f(qkv[(long)p*3*D+D+h*DH+d]); a=rb(rb(a)*0.23570226039551584f);} else a=rb(-1e9f);
    lg[w]=a; mx=fmaxf(mx,a);}
  float sum=0; for(int w=0;w<W;w++){ lg[w]=__expf(lg[w]-mx); sum+=lg[w]; }
  for(int w=0;w<W;w++) lg[w]=rb(lg[w]/sum);
  for(int d=0;d<DH;d++){ float a=0; for(int w=0;w<W;w++){ int p=s-w*DIL; int pc=p>=0?p:0; a+=lg[w]*b2f(qkv[(long)pc*3*D+2*D+h*DH+d]); } o[(long)s*D+h*DH+d]=f2b(a);}
}
__global__ void geglu_k(const bf16* g,const bf16* u,bf16* h,long n){
  long i=(long)blockIdx.x*256+threadIdx.x; if(i>=n) return;
  float x=b2f(g[i]); float t=tanhf(0.7978845608028654f*(x+0.044715f*x*x*x)); float ge=rb(0.5f*x*(1.f+t));
  h[i]=f2b(ge*b2f(u[i]));
}
__global__ void final_k(const bf16* x,const bf16* s,const bf16* d,bf16* out,long n){
  long i=(long)blockIdx.x*256+threadIdx.x; if(i>=n) return;
  float dv=b2f(d[i]); float g=rb(dv/(1.f+__expf(-dv)));
  out[i]=f2b(b2f(x[i])+rb(b2f(s[i])*g));
}
extern "C" void kernel_launch(void* const* d_in, const int* in_sizes, int n_in, void* d_out, int out_size, void* d_ws, size_t ws_size, hipStream_t stream){
  const bf16* x=(const bf16*)d_in[0]; const bf16* wqkv=(const bf16*)d_in[1]; const bf16* wo=(const bf16*)d_in[2];
  const bf16* wg=(const bf16*)d_in[4]; const bf16* wu=(const bf16*)d_in[5]; const bf16* wd=(const bf16*)d_in[6];
  bf16* out=(bf16*)d_out; char* ws=(char*)d_ws;
  bf16* xn=(bf16*)ws; ws+=(size_t)S*D*2;
  bf16* qkv=(bf16*)ws; ws+=(size_t)S*3*D*2;
  bf16* o=(bf16*)ws; ws+=(size_t)S*D*2;
  bf16* sb=(bf16*)ws; ws+=(size_t)S*D*2;
  bf16* g=(bf16*)ws; ws+=(size_t)S*F*2;
  bf16* u=(bf16*)ws; ws+=(size_t)S*F*2;
  bf16* h=(bf16*)ws; ws+=(size_t)S*F*2;
  bf16* dd=(bf16*)ws; ws+=(size_t)S*D*2;
  ln_k<<<S,64,0,stream>>>(x,xn);
  gemm_k<<<dim3(3*D/16,S/16),dim3(16,16),0,stream>>>(xn,wqkv,qkv,S,3*D,D);
  attn_k<<<S,64,0,stream>>>(qkv,o);
  gemm_k<<<dim3(D/16,S/16),dim3(16,16),0,stream>>>(o,wo,sb,S,D,D);
  gemm_k<<<dim3(F/16,S/16),dim3(16,16),0,stream>>>(xn,wg,g,S,F,D);
  gemm_k<<<dim3(F/16,S/16),dim3(16,16),0,stream>>>(xn,wu,u,S,F,D);
  geglu_k<<<(S*F+255)/256,256,0,stream>>>(g,u,h,(long)S*F);
  gemm_k<<<dim3(D/16,S/16),dim3(16,16),0,stream>>>(h,wd,dd,S,D,F);
  final_k<<<(S*D+255)/256,256,0,stream>>>(x,sb,dd,out,(long)S*D);
}
```

```cpp
#include <hip/hip_runtime.h>
#include <hip/hip_bf16.h>
#include <stdint.h>

typedef __hip_bfloat16 bf16;
typedef short bf16x8 __attribute__((ext_vector_type(8)));
typedef short s16x4 __attribute__((ext_vector_type(4)));
typedef float f32x4 __attribute__((ext_vector_type(4)));
typedef float f32x2 __attribute__((ext_vector_type(2)));
typedef __bf16 bf2 __attribute__((ext_vector_type(2)));
typedef unsigned u32x2 __attribute__((ext_vector_type(2)));
typedef unsigned u32x4 __attribute__((ext_vector_type(4)));
#define LAS __attribute__((address_space(3)))
#define DI __device__ __forceinline__

constexpr int S_ = 2048, D_ = 576, F_ = 4608, NH = 32, DH = 18, QKVN = 1728;

DI unsigned pk2(float a, float b) { f32x2 v = {a, b}; bf2 r = __builtin_convertvector(v, bf2); return __builtin_bit_cast(unsigned, r); }
DI float rbf(float a) { return __uint_as_float(pk2(a, 0.f) << 16); }
DI float lo2f(unsigned u) { return __uint_as_float(u << 16); }
DI float hi2f(unsigned u) { return __uint_as_float(u & 0xffff0000u); }

__global__ __launch_bounds__(576) void k0_ln(const bf16* __restrict__ x, bf16* __restrict__ xnf) {
  __shared__ float red[2][9][16];
  const int tid = threadIdx.x, w = tid >> 6, l = tid & 63, m = l & 15, g = l >> 4;
  const int mb = blockIdx.x;
  const char* xr = (const char*)x + (size_t)(mb * 16 + m) * (D_ * 2);
  u32x2 v[4];
#pragma unroll
  for (int t = 0; t < 2; t++) {
    int s = 2 * w + t;
    v[2 * t] = *(const u32x2*)(xr + (32 * s + 4 * g) * 2);
    v[2 * t + 1] = *(const u32x2*)(xr + (32 * s + 16 + 4 * g) * 2);
  }
  float f[16];
#pragma unroll
  for (int i = 0; i < 4; i++) { f[4 * i] = lo2f(v[i][0]); f[4 * i + 1] = hi2f(v[i][0]); f[4 * i + 2] = lo2f(v[i][1]); f[4 * i + 3] = hi2f(v[i][1]); }
  float s = 0;
#pragma unroll
  for (int i = 0; i < 16; i++) s += f[i];
  s += __shfl_xor(s, 16); s += __shfl_xor(s, 32);
  if (g == 0) red[0][w][m] = s;
  __syncthreads();
  float tot = 0;
#pragma unroll
  for (int i = 0; i < 9; i++) tot += red[0][i][m];
  const float mean = tot * (1.f / D_);
  float q = 0;
#pragma unroll
  for (int i = 0; i < 16; i++) { float d = f[i] - mean; q += d * d; }
  q += __shfl_xor(q, 16); q += __shfl_xor(q, 32);
  if (g == 0) red[1][w][m] = q;
  __syncthreads();
  float qt = 0;
#pragma unroll
  for (int i = 0; i < 9; i++) qt += red[1][i][m];
  const float mb16 = rbf(mean);
  const float var = rbf(qt * (1.f / D_));
  const float den = rbf(sqrtf(rbf(var + 1e-5f)));
  float o[16];
#pragma unroll
  for (int i = 0; i < 16; i++) o[i] = rbf(f[i] - mb16) / den;
#pragma unroll
  for (int t = 0; t < 2; t++) {
    int s2 = 2 * w + t;
    u32x4 r;
    r[0] = pk2(o[8 * t], o[8 * t + 1]); r[1] = pk2(o[8 * t + 2], o[8 * t + 3]);
    r[2] = pk2(o[8 * t + 4], o[8 * t + 5]); r[3] = pk2(o[8 * t + 6], o[8 * t + 7]);
    *(u32x4*)((char*)xnf + ((size_t)(mb * 18 + s2) * 64 + l) * 16) = r;
  }
}

constexpr int SLICE_B = 16384;
constexpr int K1_LDS = 9 * SLICE_B;

DI void glds16(const char* sbase, unsigned voff, unsigned ldsaddr) {
  asm volatile("s_mov_b32 m0, %0\n\ts_nop 0\n\tglobal_load_lds_dwordx4 %1, %2" ::"s"(ldsaddr), "v"(voff), "s"(sbase) : "memory");
}
#define WAITV(n) asm volatile("s_waitcnt vmcnt(%0)" ::"i"(n) : "memory")

DI s16x4 trread(unsigned addr) {
  return __builtin_amdgcn_ds_read_tr16_b64_v4i16((LAS s16x4*)(uintptr_t)addr);
}

struct PassDesc { const char* pA; const char* pB; unsigned strideB; int u; };

DI PassDesc make_pass(int u, int nu, const bf16* wqkv, const bf16* wg, const bf16* wu) {
  PassDesc d;
  d.u = u;
  if (u < 288) { d.pA = (const char*)(wg + u * 16); d.pB = (const char*)(wu + u * 16); d.strideB = F_ * 2; }
  else { int c = (u - 288) * 32; d.pA = (const char*)(wqkv + c); d.pB = (const char*)(wqkv + c + nu * 16); d.strideB = QKVN * 2; }
  return d;
}

DI float geglu(float g, float u) {
  const float c1 = 2.3022081985f, c2 = 0.1029432397f;
  float t = g * g;
  float p = __builtin_fmaf(t, -c2, -c1);
  float e = __builtin_amdgcn_exp2f(g * p);
  float r = __builtin_amdgcn_rcpf(1.f + e);
  return g * u * r;
}

DI unsigned stage_voff(int wave, int lane, unsigned strideB, int nu) {
  unsigned ch = ((lane & 7) >> 1) ^ ((lane >> 4) & 3);
  ch = min(ch, (unsigned)(nu - 1));
  return (wave * 8 + (lane >> 3)) * strideB + (ch << 5) + ((lane & 1) << 4);
}

#define SB() __builtin_amdgcn_sched_barrier(0)
#define MFMA(c, a, b) asm volatile("v_mfma_f32_16x16x32_bf16 %0, %1, %2, %0" : "+v"(c) : "v"(a), "v"(b))
#define MFMAA(c, a, b) asm volatile("v_mfma_f32_16x16x32_bf16 %0, %1, %2, %0" : "+a"(c) : "v"(a), "v"(b))
#define MFMA0(c, a, b) asm volatile("v_mfma_f32_16x16x32_bf16 %0, %1, %2, 0" : "=&v"(c) : "v"(a), "v"(b))
struct WF { s16x4 a0, a1, b0, b1; };
DI WF wread(unsigned a) { WF w; w.a0 = trread(a); w.a1 = trread(a + 2048); w.b0 = trread(a + 8192); w.b1 = trread(a + 8192 + 2048); return w; }

template <int NU, int POS>
DI void k1_pass(bf16x8 (&xf)[18][2], const PassDesc& cur, const PassDesc& nxt,
                unsigned voffc, unsigned voffn, unsigned ldsw, unsigned rdbase, int mt, int wave,
                bf16* hf, bf16* qkv) {
  f32x4 acc[NU][2][2];
  unsigned aA[NU];
#pragma unroll
  for (int u = 0; u < NU; u++) { aA[u] = rdbase ^ (u << 5); }
  WF wcur, wnxt;
  constexpr int NST = 18 * NU;
#pragma unroll
  for (int n = -1; n < NST; n++) {
    const int n1 = n + 1;
    if (n1 < NST) {
      const int s1 = n1 / NU, u1 = n1 % NU, i = s1 >> 1, t = s1 & 1;
      if (u1 == 0 && t == 0) {
        if (POS == 0) {
          constexpr int dummy = 0;
          int younger = 0;
          if (i <= 6) younger = (6 - i) * 6 + 8 + 2 * i;
          else younger = 12;
          asm volatile("s_waitcnt vmcnt(%4)" : "+v"(xf[2 * i][0]), "+v"(xf[2 * i][1]), "+v"(xf[2 * i + 1][0]), "+v"(xf[2 * i + 1][1]) : "i"(i <= 6 ? (6 - i) * 6 + 8 + 2 * i : 12) : "memory");
        } else if (POS == 1) { WAITV(12); }
        else { WAITV(i <= 2 ? 12 : 2 * (8 - i)); }
        __builtin_amdgcn_s_barrier();
        if (i < 2) {
          glds16(cur.pA + (size_t)(i + 7) * 64 * cur.strideB, voffc, ldsw + (i + 7) * SLICE_B);
          glds16(cur.pB + (size_t)(i + 7) * 64 * cur.strideB, voffc, ldsw + (i + 7) * SLICE_B + 8192);
        } else if (POS != 2) {
          glds16(nxt.pA + (size_t)(i - 2) * 64 * nxt.strideB, voffn, ldsw + (i - 2) * SLICE_B);
          glds16(nxt.pB + (size_t)(i - 2) * 64 * nxt.strideB, voffn, ldsw + (i - 2) * SLICE_B + 8192);
        }
        if (i % 3 == 0 && i > 0) {
#pragma unroll
          for (int u = 0; u < NU; u++) { aA[u] += 3 * SLICE_B; asm volatile("" : "+v"(aA[u])); }
        }
      }
      wnxt = wread(aA[u1] + (i % 3) * SLICE_B + t * 4096);
    }
    SB();
    if (n >= 0) {
      const int s = n / NU, u = n % NU;
      bf16x8 wa = __builtin_shufflevector(wcur.a0, wcur.a1, 0, 1, 2, 3, 4, 5, 6, 7);
      bf16x8 wb = __builtin_shufflevector(wcur.b0, wcur.b1, 0, 1, 2, 3, 4, 5, 6, 7);
      if (s == 0) {
#pragma unroll
        for (int mb = 0; mb < 2; mb++) MFMA0(acc[u][0][mb], wa, xf[s][mb]);
#pragma unroll
        for (int mb = 0; mb < 2; mb++) MFMA0(acc[u][1][mb], wb, xf[s][mb]);
      } else {
#pragma unroll
        for (int mb = 0; mb < 2; mb++) MFMA(acc[u][0][mb], wa, xf[s][mb]);
#pragma unroll
        for (int mb = 0; mb < 2; mb++) MFMA(acc[u][1][mb], wb, xf[s][mb]);
      }
      SB();
    }
    wcur = wnxt;
  }
  asm volatile("s_nop 15\n\ts_nop 3" ::: "memory");
  SB();
  const int lane = __builtin_amdgcn_mbcnt_hi(~0u, __builtin_amdgcn_mbcnt_lo(~0u, 0));
  if (cur.u < 288) {
    const unsigned vo = lane * 16;
#pragma unroll
    for (int u = 0; u < NU; u++) {
      const int U = cur.u + u;
#pragma unroll
      for (int mb = 0; mb < 2; mb++) {
        u32x2 r;
        r[0] = pk2(geglu(acc[u][0][mb][0], acc[u][1][mb][0]), geglu(acc[u][0][mb][1], acc[u][1][mb][1]));
        r[1] = pk2(geglu(acc[u][0][mb][2], acc[u][1][mb][2]), geglu(acc[u][0][mb][3], acc[u][1][mb][3]));
        const int gmb = mt * 16 + wave * 2 + mb;
        char* sb = (char*)hf + (size_t)(gmb * 144 + (U >> 1)) * 1024 + (U & 1) * 8;
        *(u32x2*)(sb + vo) = r;
      }
    }
  } else {
    const int c = (cur.u - 288) * 32;
    const unsigned vo = (lane & 15) * (QKVN * 2) + (lane >> 4) * 8;
#pragma unroll
    for (int u = 0; u < NU; u++) {
#pragma unroll
      for (int mb = 0; mb < 2; mb++) {
        char* sb = (char*)qkv + ((size_t)(mt * 256 + wave * 32 + mb * 16) * QKVN + c + u * 16) * 2;
        u32x2 r;
        r[0] = pk2(acc[u][0][mb][0], acc[u][0][mb][1]); r[1] = pk2(acc[u][0][mb][2], acc[u][0][mb][3]);
        *(u32x2*)(sb + vo) = r;
        r[0] = pk2(acc[u][1][mb][0], acc[u][1][mb][1]); r[1] = pk2(acc[u][1][mb][2], acc[u][1][mb][3]);
        *(u32x2*)(sb + NU * 32 + vo) = r;
      }
    }
  }
}

DI void gld16(bf16x8& dst, const char* sbase, unsigned voff) {
  asm volatile("global_load_dwordx4 %0, %1, %2" : "=v"(dst) : "v"(voff), "s"(sbase) : "memory");
}

__global__ __launch_bounds__(512, 2) void k1_gemm(const bf16* __restrict__ xnf, const bf16* __restrict__ wqkv,
                                                  const bf16* __restrict__ wg, const bf16* __restrict__ wu,
                                                  bf16* __restrict__ hf, bf16* __restrict__ qkv) {
  extern __shared__ __attribute__((aligned(16))) char lds[];
  const int tid = threadIdx.x, lane = tid & 63;
  const int wave = __builtin_amdgcn_readfirstlane(tid >> 6);
  const int b = blockIdx.x, xcd = b & 7, j = b >> 3;
  const int slot = xcd * 4 + (j & 3), mt = j >> 2;
  const int u0 = slot * 342 / 32, u1 = (slot + 1) * 342 / 32;
  const int nu1 = u1 - u0 - 7;
  const unsigned ldsbase = (unsigned)(uintptr_t)(LAS char*)lds;
  const unsigned ldsw = ldsbase + wave * 1024;
  const int g = lane >> 4, q = (lane & 15) >> 2, p = lane & 3;
  const unsigned rrow = 4 * g + q;
  const unsigned rdbase = ldsbase + rrow * 128 + (((rrow >> 1) & 3) << 5) + p * 8;

  const PassDesc p0 = make_pass(u0, 4, wqkv, wg, wu);
  const PassDesc p1 = make_pass(u0 + 4, nu1, wqkv, wg, wu);
  const PassDesc p2 = make_pass(u0 + 4 + nu1, 3, wqkv, wg, wu);
  const unsigned voff4 = stage_voff(wave, lane, p0.strideB, 4);
  const unsigned voff3 = stage_voff(wave, lane, p0.strideB, 3);
  const unsigned voff1 = nu1 == 4 ? voff4 : voff3;
  bf16x8 xf[18][2];
  const char* xb = (const char*)xnf + (size_t)(mt * 16 + wave * 2) * 18 * 1024;
  const unsigned xo = lane * 16;
#pragma unroll
  for (int i = 0; i < 9; i++) {
#pragma unroll
    for (int t = 0; t < 2; t++)
#pragma unroll
      for (int mb = 0; mb < 2; mb++) gld16(xf[2 * i + t][mb], xb + (mb * 18 + 2 * i + t) * 1024, xo);
    if (i < 7) {
      glds16(p0.pA + (size_t)i * 64 * p0.strideB, voff4, ldsw + i * SLICE_B);
      glds16(p0.pB + (size_t)i * 64 * p0.strideB, voff4, ldsw + i * SLICE_B + 8192);
    }
  }
  k1_pass<4, 0>(xf, p0, p1, voff4, voff1, ldsw, rdbase, mt, wave, hf, qkv);
  if (nu1 == 4) k1_pass<4, 1>(xf, p1, p2, voff1, voff3, ldsw, rdbase, mt, wave, hf, qkv);
  else k1_pass<3, 1>(xf, p1, p2, voff1, voff3, ldsw, rdbase, mt, wave, hf, qkv);
  k1_pass<3, 2>(xf, p2, p2, voff3, voff3, ldsw, rdbase, mt, wave, hf, qkv);
}

__global__ __launch_bounds__(256) void ka_attn(const bf16* __restrict__ qkv, bf16* __restrict__ of) {
  const int tid = threadIdx.x, h = tid & 31, r = tid >> 5;
  const int s = blockIdx.x * 8 + r;
  const unsigned* qp = (const unsigned*)(qkv + (size_t)s * QKVN + h * DH);
  float q[DH];
#pragma unroll
  for (int i = 0; i < 9; i++) { unsigned u = qp[i]; q[2 * i] = lo2f(u); q[2 * i + 1] = hi2f(u); }
  float lg[8]; float mx = -3.0e38f;
#pragma unroll
  for (int w = 0; w < 8; w++) {
    int p = s - 2 * w; int pc = p >= 0 ? p : 0;
    const unsigned* kp = (const unsigned*)(qkv + (size_t)pc * QKVN + D_ + h * DH);
    float a = 0;
#pragma unroll
    for (int i = 0; i < 9; i++) { unsigned u = kp[i]; a += q[2 * i] * lo2f(u) + q[2 * i + 1] * hi2f(u); }
    a = rbf(rbf(a) * 0.23570226039551584f);
    a = p >= 0 ? a : -1e9f;
    lg[w] = a; mx = fmaxf(mx, a);
  }
  float sum = 0;
#pragma unroll
  for (int w = 0; w < 8; w++) { lg[w] = __expf(lg[w] - mx); sum += lg[w]; }
  float inv = 1.f / sum;
  float o[DH];
#pragma unroll
  for (int i = 0; i < DH; i++) o[i] = 0;
#pragma unroll
  for (int w = 0; w < 8; w++) {
    int p = s - 2 * w; int pc = p >= 0 ? p : 0;
    const unsigned* vp = (const unsigned*)(qkv + (size_t)pc * QKVN + 2 * D_ + h * DH);
    float a = rbf(lg[w] * inv);
#pragma unroll
    for (int i = 0; i < 9; i++) { unsigned u = vp[i]; o[2 * i] += a * lo2f(u); o[2 * i + 1] += a * hi2f(u); }
  }
  const int mb = s >> 4, m = s & 15;
#pragma unroll
  for (int i = 0; i < 9; i++) {
    int k = h * DH + 2 * i;
    int st = k >> 5, kk = k & 31, hi = kk >> 4, g = (kk >> 2) & 3, jj = kk & 3;
    *(unsigned*)((char*)of + ((size_t)(mb * 18 + st) * 64 + g * 16 + m) * 16 + hi * 8 + jj * 2) = pk2(o[2 * i], o[2 * i + 1]);
  }
}

constexpr int K2_SLOTS = 8;
constexpr int K2_LDS = 4 * K2_SLOTS * 4096;
constexpr int K2_DEPTH = 4;


__global__ __launch_bounds__(256, 1) void k2_gemm(const bf16* __restrict__ hf, const bf16* __restrict__ of,
                                                  const bf16* __restrict__ wd, const bf16* __restrict__ wo,
                                                  const bf16* __restrict__ x, bf16* __restrict__ out) {
  extern __shared__ __attribute__((aligned(16))) char lds[];
  const int tid = threadIdx.x, lane = tid & 63;
  const int wave = __builtin_amdgcn_readfirstlane(tid >> 6);
  const int b = blockIdx.x, xcd = b & 7, j = b >> 3;
  const int t = xcd * 30 + j;
  if (t >= 234) return;
  const int mt = t / 9, nt = t % 9;
  const int mb0 = mt * 5;
  const int nmb = min(5, 128 - mb0);
  const unsigned ldsbase = (unsigned)(uintptr_t)(LAS char*)lds;
  const unsigned ldsw = ldsbase + wave * (K2_SLOTS * 4096);
  const unsigned scol = ((((lane & 7) >> 1) ^ ((lane >> 4) & 3)) << 5) + ((lane & 1) << 4);
  const unsigned voffw = (lane >> 3) * (D_ * 2) + scol;
  const int g = lane >> 4, q = (lane & 15) >> 2, p = lane & 3;
  const unsigned rrow = 4 * g + q;
  const unsigned rdb = ldsw + rrow * 128 + (((rrow >> 1) & 3) << 5) + p * 8;
  unsigned voffh[5], voffo[5];
#pragma unroll
  for (int i = 0; i < 5; i++) { int mb = mb0 + min(i, nmb - 1); voffh[i] = mb * 144 * 1024 + lane * 16; voffo[i] = mb * 18 * 1024 + lane * 16; }

  f32x4 accd[5][4], accs[5][4];
#pragma unroll
  for (int i = 0; i < 5; i++)
#pragma unroll
    for (int c = 0; c < 4; c++) { accd[i][c] = f32x4{0, 0, 0, 0}; accs[i][c] = f32x4{0, 0, 0, 0}; }

#pragma unroll
  for (int i = 0; i < 5; i++)
#pragma unroll
    for (int c = 0; c < 4; c++) { asm volatile("" : "+a"(accd[i][c]), "+a"(accs[i][c])); }
  asm volatile("s_nop 3");
  const char* wdb = (const char*)wd + nt * 128;
  const char* wob = (const char*)wo + nt * 128;
  bf16x8 hb[K2_DEPTH][5];

  auto issue = [&](int it, bf16x8 (&dst)[5], int slot) {
    if (it < 36) {
      int ks = wave + 4 * it;
      const char* wsrc = wdb + (size_t)ks * 32 * (D_ * 2);
#pragma unroll
      for (int r = 0; r < 4; r++) glds16(wsrc + r * 8 * (D_ * 2), voffw, ldsw + slot * 4096 + r * 1024);
      const char* hsrc = (const char*)hf + (size_t)ks * 1024;
#pragma unroll
      for (int i = 0; i < 5; i++) gld16(dst[i], hsrc, voffh[i]);
    } else {
      int ks = min(wave + 4 * (it - 36), 17);
      const char* wsrc = wob + (size_t)ks * 32 * (D_ * 2);
#pragma unroll
      for (int r = 0; r < 4; r++) glds16(wsrc + r * 8 * (D_ * 2), voffw, ldsw + slot * 4096 + r * 1024);
      const char* hsrc = (const char*)of + (size_t)ks * 1024;
#pragma unroll
      for (int i = 0; i < 5; i++) gld16(dst[i], hsrc, voffo[i]);
    }
  };
  auto compute = [&](f32x4 (&acc)[5][4], bf16x8 (&src)[5], int slot) {
    s16x4 a0[4], a1[4];
#pragma unroll
    for (int c = 0; c < 4; c++) {
      unsigned a = (rdb ^ (c << 5)) + slot * 4096;
      a0[c] = trread(a); a1[c] = trread(a + 2048);
    }
    SB();
#pragma unroll
    for (int c = 0; c < 4; c++) {
      bf16x8 wa = __builtin_shufflevector(a0[c], a1[c], 0, 1, 2, 3, 4, 5, 6, 7);
#pragma unroll
      for (int i = 0; i < 5; i++) MFMAA(acc[i][c], wa, src[i]);
    }
  };
#define WAITH(n, d) asm volatile("s_waitcnt vmcnt(%5)" : "+v"(hb[d][0]), "+v"(hb[d][1]), "+v"(hb[d][2]), "+v"(hb[d][3]), "+v"(hb[d][4]) : "i"(n) : "memory")
#pragma unroll
  for (int d = 0; d < K2_DEPTH - 1; d++) issue(d, hb[d], d);
  for (int it0 = 0; it0 < 36; it0 += 4) {
#pragma unroll
    for (int d = 0; d < 4; d++) {
      int it = it0 + d;
      issue(it + 3, hb[(d + 3) & 3], (it + 3) & 7);
      WAITH(27, d);
      compute(accd, hb[d], it & 7);
    }
  }
  {
    issue(39, hb[3], 39 & 7); WAITH(27, 0); compute(accs, hb[0], 36 & 7);
    issue(40, hb[0], 40 & 7); WAITH(27, 1); compute(accs, hb[1], 37 & 7);
    WAITH(18, 2); compute(accs, hb[2], 38 & 7);
    WAITH(9, 3); compute(accs, hb[3], 39 & 7);
    WAITH(0, 0); if (wave < 2) compute(accs, hb[0], 40 & 7);
  }
  asm volatile("s_nop 15\n\ts_nop 3" ::: "memory");
  SB();
  __syncthreads();
  f32x4 dsum[5], ssum[5];
#pragma unroll
  for (int rnd = 0; rnd < 2; rnd++) {
#pragma unroll
    for (int c = 0; c < 4; c++)
#pragma unroll
      for (int i = 0; i < 5; i++)
        *(f32x4*)(lds + (((c * 4 + wave) * 5 + i) * 64 + lane) * 16) = rnd == 0 ? accd[i][c] : accs[i][c];
    __syncthreads();
#pragma unroll
    for (int i = 0; i < 5; i++) {
      f32x4 v = *(f32x4*)(lds + (((wave * 4 + 0) * 5 + i) * 64 + lane) * 16);
#pragma unroll
      for (int w = 1; w < 4; w++) v += *(f32x4*)(lds + (((wave * 4 + w) * 5 + i) * 64 + lane) * 16);
      if (rnd == 0) dsum[i] = v; else ssum[i] = v;
    }
    __syncthreads();
  }
  const int m = lane & 15;
#pragma unroll
  for (int i = 0; i < 5; i++) {
    if (i < nmb) {
      const int row = (mb0 + i) * 16 + m;
      const int col = nt * 64 + wave * 16 + 4 * g;
      u32x2 xv = *(const u32x2*)((const char*)x + ((size_t)row * D_ + col) * 2);
      float xs[4] = {lo2f(xv[0]), hi2f(xv[0]), lo2f(xv[1]), hi2f(xv[1])};
      float o[4];
#pragma unroll
      for (int e = 0; e < 4; e++) {
        float d = rbf(dsum[i][e]);
        float sg = rbf(d * __builtin_amdgcn_rcpf(1.f + __expf(-d)));
        o[e] = xs[e] + rbf(rbf(ssum[i][e]) * sg);
      }
      u32x2 r; r[0] = pk2(o[0], o[1]); r[1] = pk2(o[2], o[3]);
      *(u32x2*)((char*)out + ((size_t)row * D_ + col) * 2) = r;
    }
  }
}

extern "C" void kernel_launch(void* const* d_in, const int* in_sizes, int n_in, void* d_out, int out_size, void* d_ws,
                              size_t ws_size, hipStream_t stream) {
  const bf16* x = (const bf16*)d_in[0];
  const bf16* wqkv = (const bf16*)d_in[1];
  const bf16* wo = (const bf16*)d_in[2];
  const bf16* wg = (const bf16*)d_in[4];
  const bf16* wu = (const bf16*)d_in[5];
  const bf16* wd = (const bf16*)d_in[6];
  bf16* out = (bf16*)d_out;
  char* ws = (char*)d_ws;
  bf16* xnf = (bf16*)ws; ws += (size_t)S_ * D_ * 2;
  bf16* of = (bf16*)ws; ws += (size_t)S_ * D_ * 2;
  bf16* qkv = (bf16*)ws; ws += (size_t)S_ * QKVN * 2;
  bf16* hf = (bf16*)ws; ws += (size_t)S_ * F_ * 2;
  (void)hipFuncSetAttribute((const void*)k1_gemm, hipFuncAttributeMaxDynamicSharedMemorySize, K1_LDS);
  (void)hipFuncSetAttribute((const void*)k2_gemm, hipFuncAttributeMaxDynamicSharedMemorySize, K2_LDS);
  k0_ln<<<128, 576, 0, stream>>>(x, xnf);
  k1_gemm<<<256, 512, K1_LDS, stream>>>(xnf, wqkv, wg, wu, hf, qkv);
  ka_attn<<<256, 256, 0, stream>>>(qkv, of);
  k2_gemm<<<240, 256, K2_LDS, stream>>>(hf, of, wd, wo, x, out);
}
```

```cpp
#include <hip/hip_runtime.h>
#include <hip/hip_bf16.h>
#include <stdint.h>

typedef __hip_bfloat16 bf16;
typedef short bf16x8 __attribute__((ext_vector_type(8)));
typedef short s16x4 __attribute__((ext_vector_type(4)));
typedef float f32x4 __attribute__((ext_vector_type(4)));
typedef float f32x2 __attribute__((ext_vector_type(2)));
typedef __bf16 bf2 __attribute__((ext_vector_type(2)));
typedef unsigned u32x2 __attribute__((ext_vector_type(2)));
typedef unsigned u32x4 __attribute__((ext_vector_type(4)));
#define LAS __attribute__((address_space(3)))
#define DI __device__ __forceinline__

constexpr int S_ = 2048, D_ = 576, F_ = 4608, NH = 32, DH = 18, QKVN = 1728;

DI unsigned pk2(float a, float b) { f32x2 v = {a, b}; bf2 r = __builtin_convertvector(v, bf2); return __builtin_bit_cast(unsigned, r); }
DI float rbf(float a) { return __uint_as_float(pk2(a, 0.f) << 16); }
DI float lo2f(unsigned u) { return __uint_as_float(u << 16); }
DI float hi2f(unsigned u) { return __uint_as_float(u & 0xffff0000u); }

__global__ __launch_bounds__(576) void k0_ln(const bf16* __restrict__ x, bf16* __restrict__ xnf, unsigned* __restrict__ cnt,
                                             const bf16* __restrict__ wd, const bf16* __restrict__ wo) {
  if (blockIdx.x >= 128) {
    const int pb = blockIdx.x - 128;
    const u32x4* src = (const u32x4*)wd;
    u32x4 a = {0, 0, 0, 0};
    for (int i = pb * 576 + threadIdx.x; i < F_ * D_ / 8; i += 128 * 576) a ^= src[i];
    const u32x4* src2 = (const u32x4*)wo;
    for (int i = pb * 576 + threadIdx.x; i < D_ * D_ / 8; i += 128 * 576) a ^= src2[i];
    asm volatile("" :: "v"(a));
    return;
  }
  if (threadIdx.x == 0) cnt[blockIdx.x * 32] = 0;
  __shared__ float red[2][9][16];
  const int tid = threadIdx.x, w = tid >> 6, l = tid & 63, m = l & 15, g = l >> 4;
  const int mb = blockIdx.x;
  const char* xr = (const char*)x + (size_t)(mb * 16 + m) * (D_ * 2);
  u32x2 v[4];
#pragma unroll
  for (int t = 0; t < 2; t++) {
    int s = 2 * w + t;
    v[2 * t] = *(const u32x2*)(xr + (32 * s + 4 * g) * 2);
    v[2 * t + 1] = *(const u32x2*)(xr + (32 * s + 16 + 4 * g) * 2);
  }
  float f[16];
#pragma unroll
  for (int i = 0; i < 4; i++) { f[4 * i] = lo2f(v[i][0]); f[4 * i + 1] = hi2f(v[i][0]); f[4 * i + 2] = lo2f(v[i][1]); f[4 * i + 3] = hi2f(v[i][1]); }
  float s = 0;
#pragma unroll
  for (int i = 0; i < 16; i++) s += f[i];
  s += __shfl_xor(s, 16); s += __shfl_xor(s, 32);
  if (g == 0) red[0][w][m] = s;
  __syncthreads();
  float tot = 0;
#pragma unroll
  for (int i = 0; i < 9; i++) tot += red[0][i][m];
  const float mean = tot * (1.f / D_);
  float q = 0;
#pragma unroll
  for (int i = 0; i < 16; i++) { float d = f[i] - mean; q += d * d; }
  q += __shfl_xor(q, 16); q += __shfl_xor(q, 32);
  if (g == 0) red[1][w][m] = q;
  __syncthreads();
  float qt = 0;
#pragma unroll
  for (int i = 0; i < 9; i++) qt += red[1][i][m];
  const float mb16 = rbf(mean);
  const float var = rbf(qt * (1.f / D_));
  const float den = rbf(sqrtf(rbf(var + 1e-5f)));
  float o[16];
#pragma unroll
  for (int i = 0; i < 16; i++) o[i] = rbf(f[i] - mb16) / den;
#pragma unroll
  for (int t = 0; t < 2; t++) {
    int s2 = 2 * w + t;
    u32x4 r;
    r[0] = pk2(o[8 * t], o[8 * t + 1]); r[1] = pk2(o[8 * t + 2], o[8 * t + 3]);
    r[2] = pk2(o[8 * t + 4], o[8 * t + 5]); r[3] = pk2(o[8 * t + 6], o[8 * t + 7]);
    *(u32x4*)((char*)xnf + ((size_t)(mb * 18 + s2) * 64 + l) * 16) = r;
  }
}

constexpr int SLICE_B = 16384;
constexpr int K1_LDS = 9 * SLICE_B;

DI void glds16(const char* sbase, unsigned voff, unsigned ldsaddr) {
  asm volatile("s_mov_b32 m0, %0\n\ts_nop 0\n\tglobal_load_lds_dwordx4 %1, %2" ::"s"(ldsaddr), "v"(voff), "s"(sbase) : "memory");
}
#define WAITV(n) asm volatile("s_waitcnt vmcnt(%0)" ::"i"(n) : "memory")

DI s16x4 trread(unsigned addr) {
  return __builtin_amdgcn_ds_read_tr16_b64_v4i16((LAS s16x4*)(uintptr_t)addr);
}

#define ST(idx)
#define STR(idx)
#define ST1(i)
#define STR1(i)
#define ST2(i)
#define STR2(i)
constexpr int GRP_B = 3 * SLICE_B;

DI float geglu(float g, float u) {
  const float c1 = 2.3022081985f, c2 = 0.1029432397f;
  float t = g * g;
  float p = __builtin_fmaf(t, -c2, -c1);
  float e = __builtin_amdgcn_exp2f(g * p);
  float r = __builtin_amdgcn_rcpf(1.f + e);
  return g * u * r;
}
DI int lane_id() { return __builtin_amdgcn_mbcnt_hi(~0u, __builtin_amdgcn_mbcnt_lo(~0u, 0)); }

#define SB() __builtin_amdgcn_sched_barrier(0)
#define MFMA(c, a, b) asm volatile("v_mfma_f32_16x16x32_bf16 %0, %1, %2, %0" : "+v"(c) : "v"(a), "v"(b))
#define MFMAA(c, a, b) asm volatile("v_mfma_f32_16x16x32_bf16 %0, %1, %2, %0" : "+a"(c) : "v"(a), "v"(b))
#define MFMA0(c, a, b) asm volatile("v_mfma_f32_16x16x32_bf16 %0, %1, %2, 0" : "=&v"(c) : "v"(a), "v"(b))
struct WF { s16x4 a0, a1, b0, b1; };
DI WF wread(unsigned a) { WF w; w.a0 = trread(a); w.a1 = trread(a + 2048); w.b0 = trread(a + 8192); w.b1 = trread(a + 8192 + 2048); return w; }
DI void gld16(bf16x8& dst, const char* sbase, unsigned voff) {
  asm volatile("global_load_dwordx4 %0, %1, %2" : "=v"(dst) : "v"(voff), "s"(sbase) : "memory");
}
DI void gst16(char* sbase, unsigned voff, u32x4 v) {
  asm volatile("global_store_dwordx4 %0, %1, %2\n\ts_nop 1" :: "v"(voff), "v"(v), "s"(sbase) : "memory");
}

struct K1Ctx {
  const char* pW[3];
  char* outp[3];
  const char* xb;
  unsigned ldsw, strideB;
  int upar[3];
};
constexpr int KS_B = 8192;
constexpr int HALF_B = 9 * KS_B;
#define VMUL(d, a, b) asm volatile("v_mul_f32 %0, %1, %2" : "=v"(d) : "v"(a), "v"(b))

template <bool FFN>
struct K1 {
  const K1Ctx& c;
  bf16x8 xf[18][2];
  f32x4 acc[4][2][2];
  WF wf[3];
  unsigned pk[4];
  unsigned rdbase, voff4, voff3, voffm, xo, sto, ra;
  float t0, t1, t2, t3;
  DI K1(const K1Ctx& c_) : c(c_) {}

  template <int P, int S> DI void stage() {
    const unsigned vo = P == 0 ? voff4 : (P == 1 ? voffm : voff3);
    glds16(c.pW[P] + (size_t)S * 32 * c.strideB, vo, c.ldsw + S * KS_B);
  }
  template <int S, int MB> DI void xload() { gld16(xf[S][MB], c.xb + (MB * 18 + S) * 1024, xo); }
  template <int K> DI void xwload() {
    constexpr int S = K / 3, r = K % 3;
    if constexpr (r < 2) xload<S, r>(); else stage<0, S>();
  }
  static constexpr int EPI_N = 45;
  template <int P, int U, int M> DI void epi() {
    if constexpr (FFN) {
      const float c1 = 2.3022081985f, c2 = 0.1029432397f;
      constexpr int E2 = M / 21, r = M % 21;
      if constexpr (E2 < 2) {
        constexpr int sub = r <= 10 ? 0 : 1;
        constexpr int ph = sub == 0 ? r : r - 10;
        constexpr int E = E2 * 2 + sub;
        constexpr int mb = E >> 1, h = (E & 1) * 2;
        const float ga = acc[U][0][mb][h], gb = acc[U][0][mb][h + 1], ua = acc[U][1][mb][h], ub = acc[U][1][mb][h + 1];
        if constexpr (sub == 0) {
          if constexpr (ph == 0) { VMUL(t0, ga, ga); VMUL(t1, gb, gb); }
          if constexpr (ph == 1) { t0 = __builtin_fmaf(t0, -c2, -c1); t1 = __builtin_fmaf(t1, -c2, -c1); }
          if constexpr (ph == 2) { VMUL(t0, ga, t0); VMUL(t1, gb, t1); }
          if constexpr (ph == 3) { t0 = __builtin_amdgcn_exp2f(t0); }
          if constexpr (ph == 4) { t1 = __builtin_amdgcn_exp2f(t1); }
          if constexpr (ph == 5) { t0 = 1.f + t0; asm volatile("" : "+v"(t0)); t1 = 1.f + t1; }
          if constexpr (ph == 6) { t0 = __builtin_amdgcn_rcpf(t0); }
          if constexpr (ph == 7) { t1 = __builtin_amdgcn_rcpf(t1); }
          if constexpr (ph == 8) { VMUL(t2, ga, ua); VMUL(t3, gb, ub); }
          if constexpr (ph == 9) { VMUL(t2, t2, t0); VMUL(t3, t3, t1); }
          if constexpr (ph == 10) {
            pk[E] = pk2(t2, t3);
            const float gn = acc[U][0][mb][2];
            VMUL(t0, gn, gn);
          }
        } else {
          if constexpr (ph == 1) { VMUL(t1, gb, gb); t0 = __builtin_fmaf(t0, -c2, -c1); }
          if constexpr (ph == 2) { t1 = __builtin_fmaf(t1, -c2, -c1); VMUL(t0, ga, t0); }
          if constexpr (ph == 3) { t0 = __builtin_amdgcn_exp2f(t0); }
          if constexpr (ph == 4) { VMUL(t1, gb, t1); t0 = 1.f + t0; }
          if constexpr (ph == 5) { t1 = __builtin_amdgcn_exp2f(t1); }
          if constexpr (ph == 6) { t0 = __builtin_amdgcn_rcpf(t0); }
          if constexpr (ph == 7) { t1 = 1.f + t1; VMUL(t2, ga, ua); }
          if constexpr (ph == 8) { t1 = __builtin_amdgcn_rcpf(t1); }
          if constexpr (ph == 9) { VMUL(t3, gb, ub); VMUL(t2, t2, t0); }
          if constexpr (ph == 10) { VMUL(t3, t3, t1); pk[E] = pk2(t2, t3); }
        }
        asm volatile("" : "+v"(t0), "+v"(t1), "+v"(t2), "+v"(t3));
      } else if constexpr (r == 1) {
        auto r0 = __builtin_amdgcn_permlane16_swap(pk[0], pk[2], false, false);
        auto r1 = __builtin_amdgcn_permlane16_swap(pk[1], pk[3], false, false);
        pk[0] = r0[0]; pk[2] = r0[1]; pk[1] = r1[0]; pk[3] = r1[1];
        asm volatile("" : "+v"(pk[0]), "+v"(pk[1]), "+v"(pk[2]), "+v"(pk[3]));
      } else if constexpr (r == 2) {
        u32x4 v = {pk[0], pk[1], pk[2], pk[3]};
        const int Ug = c.upar[P] + U;
        gst16(c.outp[P] + (Ug >> 1) * 1024 + (Ug & 1) * 512, sto, v);
      }
    } else {
      if constexpr (M < 10) {
        constexpr int mb = M / 5, r = M % 5;
        if constexpr (r < 2) { pk[2 * r] = pk2(acc[U][r][mb][0], acc[U][r][mb][1]); pk[2 * r + 1] = pk2(acc[U][r][mb][2], acc[U][r][mb][3]); asm volatile("" : "+v"(pk[2 * r]), "+v"(pk[2 * r + 1])); }
        if constexpr (r == 3) { u32x4 v = {pk[0], pk[1], pk[2], pk[3]}; gst16(c.outp[P] + mb * (54 * 1024) + U * 1024, sto, v); }
      }
    }
  }
  template <int NU> static constexpr int epi_start(int U) {
    int t = 0;
    for (int i = 0; i <= U; i++) { int av = 4 * (9 * (i + 1) + 1); t = (i == 0) ? av : (t + EPI_N > av ? t + EPI_N : av); }
    return t;
  }
  template <int P, int NU, int T, int U = 0> DI void epi_at() {
    if constexpr (U < NU) {
      constexpr int M = T - epi_start<NU>(U);
      if constexpr (M >= 0 && M < EPI_N) epi<P, U, M>();
      epi_at<P, NU, T, U + 1>();
    }
  }
  template <int P, int NU, int T0, int T1> DI void epi_range() {
    if constexpr (T0 < T1) { epi_at<P, NU, T0>(); epi_range<P, NU, T0 + 1, T1>(); }
  }
  template <int P, int NU> static constexpr int st_u(int n) { return (P == 0 && n < 9 * NU) ? (n % (3 * NU)) / 3 : (n % (9 * NU)) / 9; }
  template <int P, int NU> static constexpr int st_s(int n) { return (P == 0 && n < 9 * NU) ? (n / (3 * NU)) * 3 + n % 3 : (n / (9 * NU)) * 9 + n % 9; }
  template <int P, int NU> static constexpr bool st_b(int n) { return (P == 0 && n < 9 * NU) ? n % (3 * NU) == 0 : n % (9 * NU) == 0; }

  template <int S0, int NS, int W> DI void waitx() {
    if constexpr (NS == 3) {
      asm volatile("s_waitcnt vmcnt(%6) lgkmcnt(0)"
                   : "+v"(xf[S0][0]), "+v"(xf[S0][1]), "+v"(xf[S0 + 1][0]), "+v"(xf[S0 + 1][1]), "+v"(xf[S0 + 2][0]), "+v"(xf[S0 + 2][1])
                   : "i"(W) : "memory");
    } else { waitx<S0, 3, W>(); waitx<S0 + 3, 3, W>(); waitx<S0 + 6, 3, W>(); }
  }

  template <int P, int NU, int NUP, int NUN, int N> DI void step() {
    constexpr int NST = 18 * NU, H = 9 * NU;
    constexpr int n2 = N + 2;
    constexpr bool nextp = n2 >= NST;
    constexpr bool rd = !nextp || NUN > 0;
    constexpr int NU2 = nextp ? (NUN > 0 ? NUN : 1) : NU;
    constexpr int m2 = nextp ? n2 - NST : n2;
    constexpr int P2 = nextp ? P + 1 : P;
    constexpr int u2 = st_u<P2, NU2>(m2), s2 = st_s<P2, NU2>(m2);
    constexpr bool bnd = rd && st_b<P2, NU2>(m2);
    constexpr int h2 = m2 / (9 * NU2), pos = m2 % (9 * NU2);
    constexpr int reg2 = s2 / 6, ro = (s2 % 6) * KS_B;
    constexpr bool newra = rd && (m2 == 0 || st_u<P2, NU2>(m2 - 1) != u2 || st_s<P2, NU2>(m2 - 1) / 6 != reg2);
    constexpr int NN = N < 0 ? 0 : N;
    constexpr int u = st_u<P, NU>(NN), s = st_s<P, NU>(NN);
    WF& wn = wf[n2 % 3];
    const WF& w = wf[NN % 3];
    bf16x8 wa = __builtin_shufflevector(w.a0, w.a1, 0, 1, 2, 3, 4, 5, 6, 7);
    bf16x8 wb = __builtin_shufflevector(w.b0, w.b1, 0, 1, 2, 3, 4, 5, 6, 7);
    if constexpr (newra) { asm volatile("v_xor_b32 %0, %2, %1\n\tv_add_u32 %0, %3, %0" : "=&v"(ra) : "v"(rdbase), "i"(u2 << 5), "i"(reg2 * 6 * KS_B)); }
#define GAP(K, ACC, WW, XX, RD)                                                                   \
    if constexpr (N >= 0) { if constexpr (s == 0) MFMA0(ACC, WW, XX); else MFMA(ACC, WW, XX); }   \
    SB();                                                                                         \
    if constexpr (rd && !bnd) { RD; }                                                             \
    if constexpr (N >= 0 && NUP > 0 && N < H) epi_at<P - 1, NUP, 4 * (9 * NUP + N) + K>();        \
    if constexpr (N >= H) epi_at<P, NU, 4 * (N - H) + K>();                                       \
    SB();
    GAP(0, acc[u][0][0], wa, xf[s][0], wn.a0 = trread(ra + ro))
    GAP(1, acc[u][0][1], wa, xf[s][1], wn.a1 = trread(ra + ro + 2048))
    if constexpr (rd) {
      if constexpr (P2 == 0 && h2 == 0) {
        if constexpr (pos == 1) { xwload<18>(); xwload<19>(); }
        else if constexpr (pos >= 2) xwload<18 + pos>();
      } else if constexpr (pos >= 1 && pos <= 17 && (pos & 1) == 1) {
        constexpr int TP = h2 == 0 ? P2 : P2 + 1, TS = (h2 == 0 ? 9 : 0) + (pos >> 1);
        if constexpr (TP <= 2) stage<TP, TS>();
      }
    }
    GAP(2, acc[u][1][0], wb, xf[s][0], wn.b0 = trread(ra + ro + 4096))
    GAP(3, acc[u][1][1], wb, xf[s][1], wn.b1 = trread(ra + ro + 4096 + 2048))
#undef GAP
    if constexpr (bnd) {
      if constexpr (P2 == 0 && h2 == 0) {
        constexpr int sg = pos / 12;
        constexpr int issued = pos == 0 ? 18 : 18 + pos + 1;
        waitx<sg * 3, 3, issued - 9 * (sg + 1)>();
      } else if constexpr (P2 == 0) { waitx<9, 9, 0>(); }
      else { asm volatile("s_waitcnt vmcnt(0) lgkmcnt(0)" ::: "memory"); }
      ST1(2 + (P2 * 4 + h2 * 3 + pos / 12) * 2);
      asm volatile("s_barrier" ::: "memory");
      ST1(3 + (P2 * 4 + h2 * 3 + pos / 12) * 2);
      wn.a0 = trread(ra + ro); wn.a1 = trread(ra + ro + 2048); wn.b0 = trread(ra + ro + 4096); wn.b1 = trread(ra + ro + 4096 + 2048);
      SB();
    }
  }
  template <int P, int NU, int NUP, int NUN, int N0, int N1> DI void steps() {
    if constexpr (N0 < N1) { step<P, NU, NUP, NUN, N0>(); steps<P, NU, NUP, NUN, N0 + 1, N1>(); }
  }
  template <int K> DI void prologue() { if constexpr (K < 18) { xwload<K>(); prologue<K + 1>(); } }

  template <int NU1> DI void run() {
    STR1(59);
    ST1(0);
    prologue<0>();
    ST1(1);
    steps<0, 4, 0, NU1, -2, 72>();
    steps<1, NU1, 4, 3, 0, 18 * NU1>();
    steps<2, 3, NU1, 0, 0, 54>();
    ST1(61);
    asm volatile("s_nop 15\n\ts_nop 3" ::: "memory");
    SB();
    epi_range<2, 3, 4 * 27, epi_start<3>(2) + EPI_N>();
    ST1(62);
    ST1(63);
    STR1(60);
  }
};

DI unsigned stage_voff(int wave, int lane, unsigned strideB, int nu, int chstride) {
  unsigned ch = ((lane & 7) >> 1) ^ ((lane >> 4) & 3);
  ch = min(ch, (unsigned)(nu - 1));
  return ((wave & 3) * 8 + (lane >> 3)) * strideB + ch * chstride + ((lane & 1) << 4);
}

__global__ __launch_bounds__(512, 2) void k1_gemm(const bf16* __restrict__ xnf, const bf16* __restrict__ wqkv,
                                                  const bf16* __restrict__ wg, const bf16* __restrict__ wu,
                                                  bf16* __restrict__ hf, bf16* __restrict__ qkv) {
  extern __shared__ __attribute__((aligned(16))) char lds[];
  const int tid = threadIdx.x, lane = tid & 63;
  const int wave = __builtin_amdgcn_readfirstlane(tid >> 6);
  const int b = blockIdx.x, xcd = b & 7, j = b >> 3;
  const int slot = xcd * 4 + (j & 3), mt = j >> 2;
  const int u0 = slot * 342 / 32, u1 = (slot + 1) * 342 / 32;
  const int nu1 = u1 - u0 - 7;
  const unsigned ldsbase = (unsigned)(uintptr_t)(LAS char*)lds;
  const int g = lane >> 4, q = (lane & 15) >> 2, p = lane & 3;
  const unsigned rrow = 4 * g + q;
  const unsigned rdbase = ldsbase + rrow * 128 + (((rrow >> 1) & 3) << 5) + p * 8;
  const int gmb = mt * 16 + wave * 2;
  K1Ctx c;
  c.ldsw = ldsbase + wave * 1024;
  c.xb = (const char*)xnf + (size_t)gmb * 18 * 1024;
  const int us[3] = {u0, u0 + 4, u0 + 4 + nu1};
  if (u0 < 288) {
    c.strideB = F_ * 2;
    const bf16* wsel = wave < 4 ? wg : wu;
#pragma unroll
    for (int i = 0; i < 3; i++) {
      c.pW[i] = (const char*)(wsel + us[i] * 16);
      c.outp[i] = (char*)hf + (size_t)(gmb * 144 + (us[i] >> 1)) * 1024; c.upar[i] = us[i] & 1;
    }
    K1<true> k(c);
    k.rdbase = rdbase; k.xo = lane * 16;
    k.voff4 = stage_voff(wave, lane, c.strideB, 4, 32); k.voff3 = stage_voff(wave, lane, c.strideB, 3, 32);
    k.voffm = nu1 == 4 ? k.voff4 : k.voff3;
    k.sto = (g & 1) * (144 * 1024) + ((g >> 1) * 16 + (lane & 15)) * 16;
    if (nu1 == 4) k.run<4>(); else k.run<3>();
  } else {
    c.strideB = QKVN * 2;
#pragma unroll
    for (int i = 0; i < 3; i++) {
      c.pW[i] = (const char*)(wqkv + (us[i] - 288) * 32) + (wave < 4 ? 0 : 32);
      c.outp[i] = (char*)qkv + (size_t)(gmb * 54 + (us[i] - 288)) * 1024; c.upar[i] = 0;
    }
    K1<false> k(c);
    k.rdbase = rdbase; k.xo = lane * 16;
    k.voff4 = stage_voff(wave, lane, c.strideB, 4, 64); k.voff3 = stage_voff(wave, lane, c.strideB, 3, 64);
    k.voffm = nu1 == 4 ? k.voff4 : k.voff3;
    k.sto = lane * 16;
    if (nu1 == 4) k.run<4>(); else k.run<3>();
  }
}

constexpr int K2_WREG = 20480;
constexpr int K2_LDS = 8 * K2_WREG;
typedef __attribute__((address_space(1))) unsigned gu32;

DI void gst16_sc1(char* sbase, unsigned voff, u32x4 v) {
  asm volatile("global_store_dwordx4 %0, %1, %2 sc1\n\ts_nop 1" :: "v"(voff), "v"(v), "s"(sbase) : "memory");
}
DI void gld16_sc1(bf16x8& dst, const char* sbase, unsigned voff) {
  asm volatile("global_load_dwordx4 %0, %1, %2 sc1" : "=v"(dst) : "v"(voff), "s"(sbase) : "memory");
}
DI float dot2(unsigned a, unsigned b, float c) { return __builtin_amdgcn_fdot2_f32_bf16(__builtin_bit_cast(bf2, a), __builtin_bit_cast(bf2, b), c, false); }

__global__ __launch_bounds__(512, 2) void k2_gemm(const bf16* __restrict__ hf, const bf16* __restrict__ qkvt, bf16* __restrict__ of,
                                                  const bf16* __restrict__ wd, const bf16* __restrict__ wo,
                                                  const bf16* __restrict__ x, bf16* __restrict__ out, unsigned* __restrict__ cnt) {
  extern __shared__ __attribute__((aligned(16))) char lds[];
  const int tid = threadIdx.x, lane = tid & 63;
  const int wave = __builtin_amdgcn_readfirstlane(tid >> 6);
  const int w4 = wave & 3;
  const int b = blockIdx.x, xcd = b & 7, j = b >> 3;
  const int t = xcd * 30 + j;
  const bool valid = j < 30 && t < 234;
  const int mt = valid ? t / 9 : 0, nt = valid ? t % 9 : 0;
  const int mb0 = mt * 5;
  const int nmb = min(5, 128 - mb0);
  const unsigned ldsbase = (unsigned)(uintptr_t)(LAS char*)lds;
  const unsigned ldsw = ldsbase + wave * K2_WREG;
  char* ldsp = lds + wave * K2_WREG;
  const unsigned scol = ((((lane & 7) >> 1) ^ ((lane >> 4) & 1)) << 5) + ((lane & 1) << 4);
  const unsigned voffw0 = (lane >> 3) * (D_ * 2) + scol;
  const unsigned voffw1 = (lane >> 3) * (D_ * 2) + (scol ^ 64);
  const int g = lane >> 4, q = (lane & 15) >> 2, p = lane & 3, m = lane & 15;
  const unsigned rrow = 8 * g + q;
  const unsigned rdb = ldsw + rrow * 128 + ((((rrow >> 1) & 1) | ((g & 1) << 1)) << 5) + p * 8;
  STR2(59);
  ST2(0);
  f32x4 acc[5][4];
  auto zacc = [&]() {
#pragma unroll
    for (int i = 0; i < 5; i++)
#pragma unroll
      for (int c = 0; c < 4; c++) { acc[i][c] = f32x4{0, 0, 0, 0}; asm volatile("" : "+v"(acc[i][c])); }
  };
  bf16x8 hb[5][5];
  auto wstage = [&](const char* wsrc, int slot) {
#pragma unroll
    for (int r = 0; r < 4; r++) glds16(wsrc + r * 8 * (D_ * 2), (r & 1) ? voffw1 : voffw0, ldsw + slot * 4096 + r * 1024);
  };
  auto compute = [&](bf16x8 (&src)[5], int slot) {
    s16x4 a0[4], a1[4];
#pragma unroll
    for (int c = 0; c < 4; c++) {
      unsigned a = (rdb ^ (c << 5)) + slot * 4096;
      a0[c] = trread(a); a1[c] = trread(a + 512);
    }
    SB();
#pragma unroll
    for (int c = 0; c < 4; c++) {
      bf16x8 wa = __builtin_shufflevector(a0[c], a1[c], 0, 1, 2, 3, 4, 5, 6, 7);
#pragma unroll
      for (int i = 0; i < 5; i++) MFMA(acc[i][c], wa, src[i]);
    }
  };
#define WAITH(n, d) asm volatile("s_waitcnt vmcnt(%5)" : "+v"(hb[d][0]), "+v"(hb[d][1]), "+v"(hb[d][2]), "+v"(hb[d][3]), "+v"(hb[d][4]) : "i"(n) : "memory")

  if (wave < 4) {
    if (valid) {
      unsigned voffh[5];
#pragma unroll
      for (int i = 0; i < 5; i++) voffh[i] = (mb0 + min(i, nmb - 1)) * 144 * 1024 + lane * 16;
      const char* wsrc = (const char*)wd + nt * 128 + (size_t)w4 * 32 * (D_ * 2);
      const char* hsrc = (const char*)hf + w4 * 1024;
      auto issue = [&](bf16x8 (&dst)[5], int slot) {
        wstage(wsrc, slot);
#pragma unroll
        for (int i = 0; i < 5; i++) gld16(dst[i], hsrc, voffh[i]);
        wsrc += 4 * 32 * (D_ * 2); hsrc += 4096;
      };
      issue(hb[0], 0); issue(hb[1], 1); issue(hb[2], 2);
      zacc();
      ST2(1);
      for (int it0 = 0; it0 < 32; it0 += 4) {
#pragma unroll
        for (int d = 0; d < 4; d++) {
          issue(hb[(d + 3) & 3], (d + 3) & 3);
          WAITH(27, d);
          compute(hb[d], d);
        }
      }
      issue(hb[3], 3); WAITH(27, 0); compute(hb[0], 0);
      WAITH(18, 1); compute(hb[1], 1);
      WAITH(9, 2); compute(hb[2], 2);
      WAITH(0, 3); compute(hb[3], 3);
    }
  } else {
    __builtin_amdgcn_s_setprio(3);
    const int mb = b >> 1, hh = b & 1;
    const int mbp = mb > 0 ? mb - 1 : 0;
    const int c0 = 72 * (4 * hh + w4);
    const int v0 = c0 >> 5;
#pragma unroll
    for (int sec = 0; sec < 5; sec++) {
      const int smb = (sec == 1 || sec == 3) ? mbp : mb;
      const int ub = (sec == 0 ? 0 : (sec <= 2 ? 18 : 36));
#pragma unroll
      for (int v = 0; v < 4; v++)
        glds16((const char*)qkvt + (size_t)(smb * 54 + ub + min(v0 + v, 17)) * 1024, lane * 16, ldsw + (sec * 4 + v) * 1024);
    }
    int coff[9];
#pragma unroll
    for (int i = 0; i < 9; i++) {
      int cidx = c0 + g * DH + 2 * i - v0 * 32; int v = cidx >> 5, cc = cidx & 31;
      coff[i] = v * 1024 + ((cc >> 2) & 3) * 256 + (cc >> 4) * 8 + (cc & 3) * 2;
    }
    asm volatile("s_waitcnt vmcnt(0)" ::: "memory");
    ST2(1);
    unsigned qv[9];
#pragma unroll
    for (int i = 0; i < 9; i++) qv[i] = *(const unsigned*)(ldsp + coff[i] + m * 16);
    const int srow = mb * 16 + m;
    float lg[8]; float mx = -3.0e38f;
#pragma unroll
    for (int w = 0; w < 8; w++) {
      const int r = m + 16 - 2 * w;
      const char* kp = ldsp + 4096 + (r >> 4) * 4096 + (r & 15) * 16;
      float a = 0;
#pragma unroll
      for (int i = 0; i < 9; i++) a = dot2(qv[i], *(const unsigned*)(kp + coff[i]), a);
      a = rbf(rbf(a) * 0.23570226039551584f);
      a = (srow - 2 * w) >= 0 ? a : -1e9f;
      lg[w] = a; mx = fmaxf(mx, a);
    }
    float sum = 0;
#pragma unroll
    for (int w = 0; w < 8; w++) { lg[w] = __expf(lg[w] - mx); sum += lg[w]; }
    const float inv = 1.f / sum;
    float o[DH];
#pragma unroll
    for (int i = 0; i < DH; i++) o[i] = 0;
#pragma unroll
    for (int w = 0; w < 8; w++) {
      const int r = m + 16 - 2 * w;
      const char* vp = ldsp + 12288 + (r >> 4) * 4096 + (r & 15) * 16;
      const float a = rbf(lg[w] * inv);
#pragma unroll
      for (int i = 0; i < 9; i++) { unsigned u = *(const unsigned*)(vp + coff[i]); o[2 * i] += a * lo2f(u); o[2 * i + 1] += a * hi2f(u); }
    }
#pragma unroll
    for (int i = 0; i < 9; i++) {
      const int cl = g * DH + 2 * i;
      *(unsigned*)(ldsp + ((cl >> 3) * 16 + m) * 16 + (cl & 7) * 2) = pk2(o[2 * i], o[2 * i + 1]);
    }
    asm volatile("s_waitcnt lgkmcnt(0)" ::: "memory");
    {
      char* ob = (char*)of + (size_t)mb * 18 * 1024 + (c0 >> 3) * 256;
      u32x4 v0_ = *(const u32x4*)(ldsp + lane * 16), v1_ = *(const u32x4*)(ldsp + 1024 + lane * 16), v2_ = *(const u32x4*)(ldsp + 2048 + (lane & 15) * 16);
      gst16_sc1(ob, lane * 16, v0_);
      gst16_sc1(ob + 1024, lane * 16, v1_);
      if (lane < 16) gst16_sc1(ob + 2048, lane * 16, v2_);
    }
    asm volatile("s_waitcnt vmcnt(0) lgkmcnt(0)" ::: "memory");
    if (lane == 0) __hip_atomic_fetch_add((gu32*)(cnt + mb * 32), 1u, __ATOMIC_RELAXED, __HIP_MEMORY_SCOPE_AGENT);
    ST2(2);
    if (valid) {
      const char* wsrc = (const char*)wo + nt * 128 + (size_t)w4 * 32 * (D_ * 2);
      const int l5 = w4 < 2 ? 4 : 0;
#pragma unroll
      for (int i = 0; i < 4; i++) wstage(wsrc + (size_t)i * 4 * 32 * (D_ * 2), i);
      wstage(wsrc + (size_t)l5 * 4 * 32 * (D_ * 2), 4);
      zacc();
      {
        gu32* cp = (gu32*)(cnt + (mb0 + min(lane, nmb - 1)) * 32);
        int spins = 0;
        while (true) {
          unsigned v = lane < 5 ? __hip_atomic_load(cp, __ATOMIC_RELAXED, __HIP_MEMORY_SCOPE_AGENT) : 8u;
          if (__all(v >= 8u) || ++spins > (1 << 20)) break;
          __builtin_amdgcn_s_sleep(4);
        }
      }
      ST2(3);
      unsigned voffo[5];
#pragma unroll
      for (int i = 0; i < 5; i++) voffo[i] = (mb0 + min(i, nmb - 1)) * 18 * 1024 + lane * 16;
      const char* osrc = (const char*)of + w4 * 1024;
#pragma unroll
      for (int d = 0; d < 4; d++)
#pragma unroll
        for (int i = 0; i < 5; i++) gld16_sc1(hb[d][i], osrc + d * 4096, voffo[i]);
#pragma unroll
      for (int i = 0; i < 5; i++) gld16_sc1(hb[4][i], osrc + l5 * 4096, voffo[i]);
      WAITH(20, 0); compute(hb[0], 0);
      WAITH(15, 1); compute(hb[1], 1);
      WAITH(10, 2); compute(hb[2], 2);
      WAITH(5, 3); compute(hb[3], 3);
      WAITH(0, 4);
      if (w4 < 2) compute(hb[4], 4);
    }
  }
  if (!valid) return;
  ST2(4);
  const int i0 = (wave >> 2) * 3, ni = (wave >> 2) ? 2 : 3;
  const int col = nt * 64 + w4 * 16 + 4 * g;
  u32x2 xv[3];
#pragma unroll
  for (int ii = 0; ii < 3; ii++) {
    const int row = min((mb0 + i0 + ii) * 16 + m, S_ - 1);
    xv[ii] = *(const u32x2*)((const char*)x + ((size_t)row * D_ + col) * 2);
  }
  asm volatile("s_nop 15\n\ts_nop 3" ::: "memory");
  SB();
#pragma unroll
  for (int i = 0; i < 5; i++)
#pragma unroll
    for (int c = 0; c < 4; c++) *(f32x4*)(ldsp + ((i * 4 + c) * 64 + lane) * 16) = acc[i][c];
  __syncthreads();
  ST2(5);
#pragma unroll
  for (int ii = 0; ii < 3; ii++) {
    const int i = i0 + ii;
    if (ii < ni && i < nmb) {
      const char* pp = lds + ((i * 4 + w4) * 64 + lane) * 16;
      f32x4 d4 = (*(const f32x4*)(pp) + *(const f32x4*)(pp + K2_WREG)) + (*(const f32x4*)(pp + 2 * K2_WREG) + *(const f32x4*)(pp + 3 * K2_WREG));
      f32x4 s4 = (*(const f32x4*)(pp + 4 * K2_WREG) + *(const f32x4*)(pp + 5 * K2_WREG)) + (*(const f32x4*)(pp + 6 * K2_WREG) + *(const f32x4*)(pp + 7 * K2_WREG));
      const float xs[4] = {lo2f(xv[ii][0]), hi2f(xv[ii][0]), lo2f(xv[ii][1]), hi2f(xv[ii][1])};
      float o[4];
#pragma unroll
      for (int e = 0; e < 4; e++) {
        float d = rbf(d4[e]);
        float sg = rbf(d * __builtin_amdgcn_rcpf(1.f + __expf(-d)));
        o[e] = xs[e] + rbf(rbf(s4[e]) * sg);
      }
      u32x2 r; r[0] = pk2(o[0], o[1]); r[1] = pk2(o[2], o[3]);
      const int row = (mb0 + i) * 16 + m;
      *(u32x2*)((char*)out + ((size_t)row * D_ + col) * 2) = r;
    }
  }
  ST2(62);
  ST2(63);
  STR2(60);
}

extern "C" void kernel_launch(void* const* d_in, const int* in_sizes, int n_in, void* d_out, int out_size, void* d_ws,
                              size_t ws_size, hipStream_t stream) {
  const bf16* x = (const bf16*)d_in[0];
  const bf16* wqkv = (const bf16*)d_in[1];
  const bf16* wo = (const bf16*)d_in[2];
  const bf16* wg = (const bf16*)d_in[4];
  const bf16* wu = (const bf16*)d_in[5];
  const bf16* wd = (const bf16*)d_in[6];
  bf16* out = (bf16*)d_out;
  char* ws = (char*)d_ws;
  bf16* xnf = (bf16*)ws; ws += (size_t)S_ * D_ * 2;
  bf16* of = (bf16*)ws; ws += (size_t)S_ * D_ * 2;
  bf16* qkv = (bf16*)ws; ws += (size_t)S_ * QKVN * 2;
  bf16* hf = (bf16*)ws; ws += (size_t)S_ * F_ * 2;
  unsigned* cnt = (unsigned*)ws; ws += 128 * 128;
  (void)hipFuncSetAttribute((const void*)k1_gemm, hipFuncAttributeMaxDynamicSharedMemorySize, K1_LDS);
  (void)hipFuncSetAttribute((const void*)k2_gemm, hipFuncAttributeMaxDynamicSharedMemorySize, K2_LDS);
  k0_ln<<<256, 576, 0, stream>>>(x, xnf, cnt, wd, wo);
  k1_gemm<<<256, 512, K1_LDS, stream>>>(xnf, wqkv, wg, wu, hf, qkv);
  k2_gemm<<<256, 512, K2_LDS, stream>>>(hf, qkv, of, wd, wo, x, out, cnt);
}
```

```cpp
#include <hip/hip_runtime.h>
#include <hip/hip_bf16.h>
#include <stdint.h>

typedef __hip_bfloat16 bf16;
typedef short bf16x8 __attribute__((ext_vector_type(8)));
typedef short s16x4 __attribute__((ext_vector_type(4)));
typedef float f32x4 __attribute__((ext_vector_type(4)));
typedef float f32x16 __attribute__((ext_vector_type(16)));
typedef float f32x2 __attribute__((ext_vector_type(2)));
typedef __bf16 bf2 __attribute__((ext_vector_type(2)));
typedef unsigned u32x2 __attribute__((ext_vector_type(2)));
typedef unsigned u32x4 __attribute__((ext_vector_type(4)));
#define LAS __attribute__((address_space(3)))
#define DI __device__ __forceinline__

#define NB0 40
#define NB1 52
#define NS0 24
#define NS1 32
#define NS2 36
constexpr int S_ = 2048, D_ = 576, F_ = 4608, DH = 18, QKVN = 1728;

DI unsigned pk2(float a, float b) { f32x2 v = {a, b}; bf2 r = __builtin_convertvector(v, bf2); return __builtin_bit_cast(unsigned, r); }
DI float rbf(float a) { return __uint_as_float(pk2(a, 0.f) << 16); }
DI float lo2f(unsigned u) { return __uint_as_float(u << 16); }
DI float hi2f(unsigned u) { return __uint_as_float(u & 0xffff0000u); }

#define ST(idx)
#define STR(idx)
#define ACC_T(i)

__global__ __launch_bounds__(256) void k0_ln(const bf16* __restrict__ x, bf16* __restrict__ xnf, unsigned* __restrict__ cnt,
                                             const bf16* __restrict__ wg, const bf16* __restrict__ wu, const bf16* __restrict__ wqkv) {
  const int tid = threadIdx.x;
  if (blockIdx.x >= 128) {
    const int pb = blockIdx.x - 128, xcd = pb & 7, jb = pb >> 3;
    const int r0 = jb * 36 + (tid >> 4), sl = xcd * 4 + ((tid >> 2) & 3), e = tid & 3;
    const int u0 = sl * 342 / 32;
    const char* base; unsigned stride;
    if (u0 < 288) { base = (const char*)((e & 2) ? wu : wg) + u0 * 32 + (e & 1) * 124; stride = F_ * 2; }
    else { base = (const char*)wqkv + (u0 - 288) * 64 + min(e * 128, 252); stride = QKVN * 2; }
    unsigned v[3];
#pragma unroll
    for (int i = 0; i < 3; i++) v[i] = *(const unsigned*)(base + (size_t)min(r0 + i * 16, jb * 36 + 35) * stride);
    unsigned acc = v[0] ^ v[1] ^ v[2];
    asm volatile("" :: "v"(acc));
    return;
  }
  if (tid < 3) cnt[(blockIdx.x * 3 + tid) * 32] = 0;
  const int row = blockIdx.x * 16 + (tid >> 4), c = tid & 15;
  const char* xr = (const char*)x + (size_t)row * (D_ * 2) + c * 8;
  u32x2 v[9];
#pragma unroll
  for (int i = 0; i < 9; i++) v[i] = *(const u32x2*)(xr + i * 128);
  float f[36];
#pragma unroll
  for (int i = 0; i < 9; i++) { f[4 * i] = lo2f(v[i][0]); f[4 * i + 1] = hi2f(v[i][0]); f[4 * i + 2] = lo2f(v[i][1]); f[4 * i + 3] = hi2f(v[i][1]); }
  float s = 0;
#pragma unroll
  for (int i = 0; i < 36; i++) s += f[i];
  s += __shfl_xor(s, 1); s += __shfl_xor(s, 2); s += __shfl_xor(s, 4); s += __shfl_xor(s, 8);
  const float mean = s * (1.f / D_);
  float q = 0;
#pragma unroll
  for (int i = 0; i < 36; i++) { float d = f[i] - mean; q += d * d; }
  q += __shfl_xor(q, 1); q += __shfl_xor(q, 2); q += __shfl_xor(q, 4); q += __shfl_xor(q, 8);
  const float mb16 = rbf(mean);
  const float var = rbf(q * (1.f / D_));
  const float den = rbf(sqrtf(rbf(var + 1e-5f)));
  const float inv = 1.f / den;
  char* ob = (char*)xnf + (size_t)blockIdx.x * 18 * 1024 + (c >> 3) * 1024 + ((c & 3) * 16 + (row & 15)) * 16 + ((c >> 2) & 1) * 8;
#pragma unroll
  for (int i = 0; i < 9; i++) {
    u32x2 r;
    r[0] = pk2(rbf(f[4 * i] - mb16) * inv, rbf(f[4 * i + 1] - mb16) * inv);
    r[1] = pk2(rbf(f[4 * i + 2] - mb16) * inv, rbf(f[4 * i + 3] - mb16) * inv);
    *(u32x2*)(ob + i * 2048) = r;
  }
}

DI void glds16(const char* sbase, unsigned voff, unsigned ldsaddr) {
  asm volatile("s_mov_b32 m0, %0\n\ts_nop 0\n\tglobal_load_lds_dwordx4 %1, %2" ::"s"(ldsaddr), "v"(voff), "s"(sbase) : "memory");
}
DI void glds16_sc1(const char* sbase, unsigned voff, unsigned ldsaddr) {
  asm volatile("s_mov_b32 m0, %0\n\ts_nop 0\n\tglobal_load_lds_dwordx4 %1, %2 sc1" ::"s"(ldsaddr), "v"(voff), "s"(sbase) : "memory");
}
DI void gld16(bf16x8& dst, const char* sbase, unsigned voff) {
  asm volatile("global_load_dwordx4 %0, %1, %2" : "=v"(dst) : "v"(voff), "s"(sbase) : "memory");
}
DI void gld16_sc1(bf16x8& dst, const char* sbase, unsigned voff) {
  asm volatile("global_load_dwordx4 %0, %1, %2 sc1" : "=v"(dst) : "v"(voff), "s"(sbase) : "memory");
}
DI void gst16_sc1(char* sbase, unsigned voff, u32x4 v) {
  asm volatile("global_store_dwordx4 %0, %1, %2 sc1\n\ts_nop 1" :: "v"(voff), "v"(v), "s"(sbase) : "memory");
}
DI void gst16_sc1_m(char* sbase, unsigned voff, u32x4 v, unsigned long long mask) {
  asm volatile("s_mov_b64 exec, %3\n\tglobal_store_dwordx4 %0, %1, %2 sc1\n\ts_mov_b64 exec, -1\n\ts_nop 0" :: "v"(voff), "v"(v), "s"(sbase), "s"(mask) : "memory");
}
DI s16x4 trread(unsigned addr) { return __builtin_amdgcn_ds_read_tr16_b64_v4i16((LAS s16x4*)(uintptr_t)addr); }
DI bf16x8 cat8(s16x4 a, s16x4 b) { return __builtin_shufflevector(a, b, 0, 1, 2, 3, 4, 5, 6, 7); }
DI float dot2(unsigned a, unsigned b, float c) { return __builtin_amdgcn_fdot2_f32_bf16(__builtin_bit_cast(bf2, a), __builtin_bit_cast(bf2, b), c, false); }
template <class T> DI T* uni(T* p) {
  unsigned long long v = (unsigned long long)p;
  unsigned lo = __builtin_amdgcn_readfirstlane((unsigned)v), hi = __builtin_amdgcn_readfirstlane((unsigned)(v >> 32));
  return (T*)(((unsigned long long)hi << 32) | lo);
}
DI int fresh_lane() {
  int l;
  asm volatile("v_mbcnt_lo_u32_b32 %0, -1, 0\n\tv_mbcnt_hi_u32_b32 %0, -1, %0" : "=v"(l));
  return l;
}
#define SB() __builtin_amdgcn_sched_barrier(0)
#define MFMA(c, a, b) asm volatile("v_mfma_f32_16x16x32_bf16 %0, %1, %2, %0" : "+v"(c) : "v"(a), "v"(b))
#define MFMA0(c, a, b) asm volatile("v_mfma_f32_16x16x32_bf16 %0, %1, %2, 0" : "=&v"(c) : "v"(a), "v"(b))
#define MFMA32A(c, a, b) asm volatile("v_mfma_f32_32x32x16_bf16 %0, %1, %2, %0" : "+v"(c) : "v"(a), "a"(b))
#define MFMA32AZ(c, a, b) asm volatile("v_mfma_f32_32x32x16_bf16 %0, %1, %2, 0" : "=&v"(c) : "v"(a), "a"(b))
DI void gld16a(bf16x8& dst, const char* sbase, unsigned voff) {
  asm volatile("global_load_dwordx4 %0, %1, %2" : "=a"(dst) : "v"(voff), "s"(sbase) : "memory");
}
constexpr int NXA = 32;
#define MFMA32(c, a, b) asm volatile("v_mfma_f32_32x32x16_bf16 %0, %1, %2, %0" : "+v"(c) : "v"(a), "v"(b))
#define MFMA32Z(c, a, b) asm volatile("v_mfma_f32_32x32x16_bf16 %0, %1, %2, 0" : "=&v"(c) : "v"(a), "v"(b))
#define VMUL(d, a, b) asm volatile("v_mul_f32 %0, %1, %2" : "=v"(d) : "v"(a), "v"(b))
#define MFMA_DRAIN() do { asm volatile("s_nop 15\n\ts_nop 7" ::: "memory"); SB(); } while (0)

DI void lds_add1(unsigned vaddr) {
  unsigned t;
  asm volatile("s_mov_b64 exec, 1\n\tv_mov_b32 %0, 1\n\tds_add_u32 %1, %0\n\ts_mov_b64 exec, -1" : "=&v"(t) : "v"(vaddr) : "memory");
}
template <int OFF> DI void lds_wait(unsigned vaddr, unsigned target) {
  unsigned t; unsigned sc, sp;
  asm volatile(
      "s_mov_b32 %2, 0\n"
      "Lsb%=:\n\t"
      "ds_read_b32 %0, %3 offset:%5\n\t"
      "s_add_u32 %2, %2, 1\n\t"
      "s_cmp_gt_u32 %2, 0x100000\n\t"
      "s_waitcnt lgkmcnt(0)\n\t"
      "s_cbranch_scc1 Lse%=\n\t"
      "v_readfirstlane_b32 %1, %0\n\t"
      "s_cmp_lt_u32 %1, %4\n\t"
      "s_cbranch_scc1 Lsb%=\n"
      "Lse%=:\n\t"
      : "=&v"(t), "=&s"(sc), "=&s"(sp) : "v"(vaddr), "s"(target), "i"(OFF) : "memory", "scc");
}
template <int OFF> DI void lds_wait_if(unsigned have, unsigned vaddr, unsigned target) {
  unsigned t; unsigned sc, sp;
  asm volatile(
      "s_cmp_ge_u32 %6, %4\n\t"
      "s_cbranch_scc1 Lse%=\n\t"
      "s_mov_b32 %2, 0\n"
      "Lsb%=:\n\t"
      "ds_read_b32 %0, %3 offset:%5\n\t"
      "s_add_u32 %2, %2, 1\n\t"
      "s_cmp_gt_u32 %2, 0x100000\n\t"
      "s_waitcnt lgkmcnt(0)\n\t"
      "s_cbranch_scc1 Lse%=\n\t"
      "v_readfirstlane_b32 %1, %0\n\t"
      "s_cmp_lt_u32 %1, %4\n\t"
      "s_cbranch_scc1 Lsb%=\n"
      "Lse%=:\n\t"
      : "=&v"(t), "=&s"(sc), "=&s"(sp) : "v"(vaddr), "s"(target), "i"(OFF), "s"(have) : "memory", "scc");
}
DI void publish(char* sbase) {
  unsigned z, o;
  asm volatile(
      "s_mov_b64 exec, 1\n\t"
      "v_mov_b32 %0, 0\n\t"
      "v_mov_b32 %1, 1\n\t"
      "global_atomic_add %0, %1, %2 sc1\n\t"
      "s_mov_b64 exec, -1\n\t"
      : "=&v"(z), "=&v"(o) : "s"(sbase) : "memory");
}
DI void pollge(int need, const char* sbase, unsigned voff, unsigned target) {
  unsigned t; unsigned sp;
  asm volatile(
      "s_cmp_eq_u32 %2, 0\n\t"
      "s_cbranch_scc1 Lpe%=\n\t"
      "s_mov_b32 %1, 0\n"
      "Lpl%=:\n\t"
      "global_load_dword %0, %3, %4 sc1\n\t"
      "s_waitcnt vmcnt(0)\n\t"
      "v_cmp_gt_u32 vcc, %5, %0\n\t"
      "s_cbranch_vccz Lpa%=\n\t"
      "s_sleep 8\n\t"
      "s_add_u32 %1, %1, 1\n\t"
      "s_cmp_lt_u32 %1, 0x20000\n\t"
      "s_cbranch_scc1 Lpl%=\n"
      "Lpa%=:\n\t"
      "Lpe%=:\n\t"
      : "=&v"(t), "=&s"(sp) : "s"(need), "v"(voff), "s"(sbase), "s"(target) : "memory", "scc", "vcc");
}

DI void glds16_if(int on, const char* sbase, unsigned voff, unsigned ldsaddr) {
  asm volatile("s_cmp_eq_u32 %3, 0\n\ts_cbranch_scc1 Lg%=\n\ts_mov_b32 m0, %0\n\ts_nop 0\n\tglobal_load_lds_dwordx4 %1, %2\nLg%=:" ::"s"(ldsaddr), "v"(voff), "s"(sbase), "s"(on) : "memory", "scc");
}
__constant__ const unsigned kOrder[144] = {
0,1,5,6,11,16,17,21,22,27,32,33,37,38,43,48,49,53,54,59,64,65,69,70,75,80,81,86,91,96,97,102,107,112,113,118,123,128,129,134,139,258,263,264,268,269,274,279,280,284,285,290,295,296,300,301,306,311,312,316,317,322,327,328,332,333,338,339,343,344,348,349,354,355,359,360,364,365,370,371,375,376,380,381,386,387,391,392,396,397,515,516,521,522,526,527,531,532,537,538,542,543,547,548,553,554,558,559,563,564,569,570,574,575,579,580,585,586,590,591,596,597,601,602,606,607,612,613,617,618,622,623,628,629,633,634,638,639,644,645,649,650,654,655
};

struct K1Ctx {
  const char* pW[3];
  char* outp[3];
  const char* xb;
  unsigned ldsw, strideB;
  int upar[3];
  char* flag;
  int flagst;
  const char* wdb; const char* wd; const char* wo; int w4, tidx, trow; unsigned tlds, tdelta; const char* tbase;
  unsigned prelds;
  int dopre;
};
constexpr int KS_B = 8192;
constexpr int HALF_B = 9 * KS_B;
struct WF { s16x4 a0, a1, b0, b1; };

template <bool FFN>
struct K1 {
  const K1Ctx& c;
  bf16x8 xf[18][2];
  f32x4 acc[4][2][2];
  WF wf[3];
  unsigned pk[4];
  unsigned rdbase, voff4, voff3, voffm, xo, sto, ra, donev;
  float t0, t1, t2, t3;
  const char* pre[3];
  DI K1(const K1Ctx& c_) : c(c_) {}

  template <int P, int S> DI void stage() {
    const unsigned vo = P == 0 ? voff4 : (P == 1 ? voffm : voff3);
    glds16(c.pW[P] + (size_t)S * 32 * c.strideB, vo, c.ldsw + S * KS_B);
  }
  template <int S, int MB> DI void xload() { gld16(xf[S][MB], c.xb + (MB * 18 + S) * 1024, xo); }
  template <int K> DI void xwload() {
    constexpr int S = K / 3, r = K % 3;
    if constexpr (r < 2) xload<S, r>(); else stage<0, S>();
  }
  static constexpr int EPI_N = 45;
  template <int P, int U, int M> DI void epi() {
    if constexpr (FFN) {
      const float c1 = 2.3022081985f, c2 = 0.1029432397f;
      constexpr int E2 = M / 21, r = M % 21;
      if constexpr (E2 < 2) {
        constexpr int sub = r <= 10 ? 0 : 1;
        constexpr int ph = sub == 0 ? r : r - 10;
        constexpr int E = E2 * 2 + sub;
        constexpr int mb = E >> 1, h = (E & 1) * 2;
        const float ga = acc[U][0][mb][h], gb = acc[U][0][mb][h + 1], ua = acc[U][1][mb][h], ub = acc[U][1][mb][h + 1];
        if constexpr (sub == 0) {
          if constexpr (ph == 0) { VMUL(t0, ga, ga); VMUL(t1, gb, gb); }
          if constexpr (ph == 1) { t0 = __builtin_fmaf(t0, -c2, -c1); t1 = __builtin_fmaf(t1, -c2, -c1); }
          if constexpr (ph == 2) { VMUL(t0, ga, t0); VMUL(t1, gb, t1); }
          if constexpr (ph == 3) { t0 = __builtin_amdgcn_exp2f(t0); }
          if constexpr (ph == 4) { t1 = __builtin_amdgcn_exp2f(t1); }
          if constexpr (ph == 5) { t0 = 1.f + t0; asm volatile("" : "+v"(t0)); t1 = 1.f + t1; }
          if constexpr (ph == 6) { t0 = __builtin_amdgcn_rcpf(t0); }
          if constexpr (ph == 7) { t1 = __builtin_amdgcn_rcpf(t1); }
          if constexpr (ph == 8) { VMUL(t2, ga, ua); VMUL(t3, gb, ub); }
          if constexpr (ph == 9) { VMUL(t2, t2, t0); VMUL(t3, t3, t1); }
          if constexpr (ph == 10) {
            pk[E] = pk2(t2, t3);
            const float gn = acc[U][0][mb][2];
            VMUL(t0, gn, gn);
          }
        } else {
          if constexpr (ph == 1) { VMUL(t1, gb, gb); t0 = __builtin_fmaf(t0, -c2, -c1); }
          if constexpr (ph == 2) { t1 = __builtin_fmaf(t1, -c2, -c1); VMUL(t0, ga, t0); }
          if constexpr (ph == 3) { t0 = __builtin_amdgcn_exp2f(t0); }
          if constexpr (ph == 4) { VMUL(t1, gb, t1); t0 = 1.f + t0; }
          if constexpr (ph == 5) { t1 = __builtin_amdgcn_exp2f(t1); }
          if constexpr (ph == 6) { t0 = __builtin_amdgcn_rcpf(t0); }
          if constexpr (ph == 7) { t1 = 1.f + t1; VMUL(t2, ga, ua); }
          if constexpr (ph == 8) { t1 = __builtin_amdgcn_rcpf(t1); }
          if constexpr (ph == 9) { VMUL(t3, gb, ub); VMUL(t2, t2, t0); }
          if constexpr (ph == 10) { VMUL(t3, t3, t1); pk[E] = pk2(t2, t3); }
        }
        asm volatile("" : "+v"(t0), "+v"(t1), "+v"(t2), "+v"(t3));
      } else if constexpr (r == 1) {
        auto r0 = __builtin_amdgcn_permlane16_swap(pk[0], pk[2], false, false);
        auto r1 = __builtin_amdgcn_permlane16_swap(pk[1], pk[3], false, false);
        pk[0] = r0[0]; pk[2] = r0[1]; pk[1] = r1[0]; pk[3] = r1[1];
        asm volatile("" : "+v"(pk[0]), "+v"(pk[1]), "+v"(pk[2]), "+v"(pk[3]));
      } else if constexpr (r == 2) {
        u32x4 v = {pk[0], pk[1], pk[2], pk[3]};
        const int Ug = c.upar[P] + U;
        gst16_sc1(c.outp[P] + (Ug >> 1) * 1024 + (Ug & 1) * 512, sto, v);
      }
    } else {
      if constexpr (M < 10) {
        constexpr int mb = M / 5, r = M % 5;
        if constexpr (r < 2) { pk[2 * r] = pk2(acc[U][r][mb][0], acc[U][r][mb][1]); pk[2 * r + 1] = pk2(acc[U][r][mb][2], acc[U][r][mb][3]); asm volatile("" : "+v"(pk[2 * r]), "+v"(pk[2 * r + 1])); }
        if constexpr (r == 3) { u32x4 v = {pk[0], pk[1], pk[2], pk[3]}; gst16_sc1(c.outp[P] + mb * (54 * 1024) + U * 1024, sto, v); }
      }
    }
  }
  template <int NU> static constexpr int epi_start(int U) {
    int t = 0;
    for (int i = 0; i <= U; i++) { int av = 4 * (9 * (i + 1) + 1); t = (i == 0) ? av : (t + EPI_N > av ? t + EPI_N : av); }
    return t;
  }
  template <int NU> static constexpr int nst(int T0, int T1) {
    int n = 0;
    for (int U = 0; U < NU; U++) {
      if (FFN) { int t = epi_start<NU>(U) + 44; n += (t >= T0 && t < T1); }
      else { int t = epi_start<NU>(U) + 3; n += (t >= T0 && t < T1); t += 5; n += (t >= T0 && t < T1); }
    }
    return n;
  }
  template <int P, int NU, int T, int U = 0> DI void epi_at() {
    if constexpr (U < NU) {
      constexpr int M = T - epi_start<NU>(U);
      if constexpr (M >= 0 && M < EPI_N) epi<P, U, M>();
      epi_at<P, NU, T, U + 1>();
    }
  }
  template <int P, int NU, int T0, int T1> DI void epi_range() {
    if constexpr (T0 < T1) { epi_at<P, NU, T0>(); epi_range<P, NU, T0 + 1, T1>(); }
  }
  template <int P, int NU> static constexpr int st_u(int n) { return (P == 0 && n < 9 * NU) ? (n % (3 * NU)) / 3 : (n % (9 * NU)) / 9; }
  template <int P, int NU> static constexpr int st_s(int n) { return (P == 0 && n < 9 * NU) ? (n / (3 * NU)) * 3 + n % 3 : (n / (9 * NU)) * 9 + n % 9; }
  template <int P, int NU> static constexpr bool st_b(int n) { return (P == 0 && n < 9 * NU) ? n % (3 * NU) == 0 : n % (9 * NU) == 0; }

  template <int S0, int NS, int W> DI void waitx() {
    if constexpr (NS == 3) {
      asm volatile("s_waitcnt vmcnt(%6) lgkmcnt(0)"
                   : "+v"(xf[S0][0]), "+v"(xf[S0][1]), "+v"(xf[S0 + 1][0]), "+v"(xf[S0 + 1][1]), "+v"(xf[S0 + 2][0]), "+v"(xf[S0 + 2][1])
                   : "i"(W) : "memory");
    } else { waitx<S0, 3, W>(); waitx<S0 + 3, 3, W>(); waitx<S0 + 6, 3, W>(); }
  }

  template <int P, int NU, int NUP, int NUN, int N> DI void step() {
    constexpr int NST = 18 * NU, H = 9 * NU;
    constexpr int n2 = N + 2;
    constexpr bool nextp = n2 >= NST;
    constexpr bool rd = !nextp || NUN > 0;
    constexpr int NU2 = nextp ? (NUN > 0 ? NUN : 1) : NU;
    constexpr int m2 = nextp ? n2 - NST : n2;
    constexpr int P2 = nextp ? P + 1 : P;
    constexpr int u2 = st_u<P2, NU2>(m2), s2 = st_s<P2, NU2>(m2);
    constexpr bool bnd = rd && st_b<P2, NU2>(m2);
    constexpr int h2 = m2 / (9 * NU2), pos = m2 % (9 * NU2);
    constexpr int reg2 = s2 / 6, ro = (s2 % 6) * KS_B;
    constexpr bool newra = rd && (m2 == 0 || st_u<P2, NU2>(m2 - 1) != u2 || st_s<P2, NU2>(m2 - 1) / 6 != reg2);
    constexpr int NN = N < 0 ? 0 : N;
    constexpr int u = st_u<P, NU>(NN), s = st_s<P, NU>(NN);
    WF& wn = wf[n2 % 3];
    const WF& w = wf[NN % 3];
    bf16x8 wa = __builtin_shufflevector(w.a0, w.a1, 0, 1, 2, 3, 4, 5, 6, 7);
    bf16x8 wb = __builtin_shufflevector(w.b0, w.b1, 0, 1, 2, 3, 4, 5, 6, 7);
    if constexpr (newra) { asm volatile("v_xor_b32 %0, %2, %1\n\tv_add_u32 %0, %3, %0" : "=&v"(ra) : "v"(rdbase), "i"(u2 << 5), "i"(reg2 * 6 * KS_B)); }
#define GAP(K, ACC, WW, XX, RD)                                                                   \
    if constexpr (N >= 0) { if constexpr (s == 0) MFMA0(ACC, WW, XX); else MFMA(ACC, WW, XX); }   \
    SB();                                                                                         \
    if constexpr (rd && !bnd) { RD; }                                                             \
    if constexpr (N >= 0 && NUP > 0 && N < H) epi_at<P - 1, NUP, 4 * (9 * NUP + N) + K>();        \
    if constexpr (N >= H) epi_at<P, NU, 4 * (N - H) + K>();                                       \
    SB();
    GAP(0, acc[u][0][0], wa, xf[s][0], wn.a0 = trread(ra + ro))
    GAP(1, acc[u][0][1], wa, xf[s][1], wn.a1 = trread(ra + ro + 2048))
    if constexpr (rd) {
      if constexpr (P2 == 0 && h2 == 0) {
        if constexpr (pos == 1) { xwload<18>(); xwload<19>(); }
        else if constexpr (pos >= 2) xwload<18 + pos>();
      } else if constexpr (pos >= 1 && pos <= 17 && (pos & 1) == 1) {
        constexpr int TP = h2 == 0 ? P2 : P2 + 1, TS = (h2 == 0 ? 9 : 0) + (pos >> 1);
        if constexpr (TP <= 2) stage<TP, TS>();
      }
      if constexpr (P2 == 2 && h2 == 1 && pos >= 1 && pos <= 12) {
        constexpr int k = pos - 1;
        glds16_if(c.dopre, pre[k >> 2] + (k & 3) * 8 * (D_ * 2), (k & 1) ? voffm : voff4, c.prelds + k * 1024);
      }
      if constexpr (P2 == 0 && h2 == 1 && pos == 20) {
        const int l = fresh_lane();
        const unsigned off = (unsigned)min(c.tidx * 21 + min(l, 20), 41471) * 128u;
        asm volatile("s_mov_b32 m0, %0\n\ts_nop 0\n\tglobal_load_lds_dword %1, %2" ::"s"(c.tlds), "v"(off), "s"(c.wd) : "memory");
        const unsigned off2 = (unsigned)min(c.tidx * 3 + min(l, 2), 5183) * 128u;
        asm volatile("s_mov_b32 m0, %0\n\ts_nop 0\n\tglobal_load_lds_dword %1, %2" ::"s"(c.tlds), "v"(off2), "s"(c.wo) : "memory");
      }
      if constexpr (P2 == 2 && h2 == 0 && pos == 18) {
        int w = c.w4;
        asm volatile("" : "+s"(w));
#pragma unroll
        for (int i = 0; i < 3; i++) pre[i] = c.wdb + (size_t)(kOrder[4 * i + w] & 255) * 32 * (D_ * 2);
      }
      if constexpr (P2 == 2 && h2 == 0 && pos == 20) {
        const int l = fresh_lane();
        const unsigned scol = ((((l & 7) >> 1) ^ ((l >> 4) & 1)) << 5) + ((l & 1) << 4);
        voff4 = (l >> 3) * (D_ * 2) + scol; voffm = (l >> 3) * (D_ * 2) + (scol ^ 64);
      }
    }
    GAP(2, acc[u][1][0], wb, xf[s][0], wn.b0 = trread(ra + ro + 4096))
    GAP(3, acc[u][1][1], wb, xf[s][1], wn.b1 = trread(ra + ro + 4096 + 2048))
#undef GAP
    if constexpr (bnd) {
      if constexpr (P2 == 0 && h2 == 0) {
        constexpr int sg = pos / 12;
        constexpr int issued = pos == 0 ? 18 : 18 + pos + 1;
        waitx<sg * 3, 3, issued - 9 * (sg + 1) + (sg < 2 ? 1 : 0)>();
      } else if constexpr (P2 == 0) { waitx<9, 9, 0>(); }
      else if constexpr (h2 == 0) { asm volatile("s_waitcnt vmcnt(%0) lgkmcnt(0)" ::"i"(nst<NU>(62, 4 * (H - 1)) + (P2 == 1 ? 2 : 0)) : "memory"); }
      else { asm volatile("s_waitcnt vmcnt(0) lgkmcnt(0)" ::: "memory"); if constexpr (FFN) publish(c.flag + (P2 - 1) * 128); }
      ST(2 + (P2 * 4 + h2 * 3 + pos / 12) * 2);
      asm volatile("s_barrier" ::: "memory");
      ST(3 + (P2 * 4 + h2 * 3 + pos / 12) * 2);
      wn.a0 = trread(ra + ro); wn.a1 = trread(ra + ro + 2048); wn.b0 = trread(ra + ro + 4096); wn.b1 = trread(ra + ro + 4096 + 2048);
      SB();
    }
  }
  template <int P, int NU, int NUP, int NUN, int N0, int N1> DI void steps() {
    if constexpr (N0 < N1) { step<P, NU, NUP, NUN, N0>(); steps<P, NU, NUP, NUN, N0 + 1, N1>(); }
  }
  template <int K> DI void prologue() { if constexpr (K < 18) { xwload<K>(); prologue<K + 1>(); } }

  template <int NU1> DI void run() {
    prologue<0>();
    {
      const int l = fresh_lane();
      const int r = c.trow + min(l / 7, 8), e = l % 7;
      unsigned off;
      if constexpr (FFN) off = r * c.strideB + min((e >> 1) * 128, NU1 * 32 + 92) + (e & 1) * c.tdelta;
      else off = r * c.strideB + min(e * 128, NU1 * 64 + 188);
      asm volatile("s_mov_b32 m0, %0\n\ts_nop 0\n\tglobal_load_lds_dword %1, %2" ::"s"(c.tlds), "v"(off), "s"(c.tbase) : "memory");
    }
    ST(1);
    steps<0, 4, 0, NU1, -2, 72>();
    steps<1, NU1, 4, 3, 0, 18 * NU1>();
    steps<2, 3, NU1, 0, 0, 54>();
    lds_add1(donev);
    ST(30);
    MFMA_DRAIN();
    epi_range<2, 3, 4 * 27, epi_start<3>(2) + EPI_N>();
    ST(31);
  }
};

DI unsigned stage_voff(int wave, int lane, unsigned strideB, int nu, int chstride) {
  unsigned ch = ((lane & 7) >> 1) ^ ((lane >> 4) & 3);
  ch = min(ch, (unsigned)(nu - 1));
  return ((wave & 3) * 8 + (lane >> 3)) * strideB + ch * chstride + ((lane & 1) << 4);
}

struct KFArgs {
  const bf16* xnf; const bf16* wqkv; const bf16* wg; const bf16* wu; const bf16* wd; const bf16* wo; const bf16* x;
  bf16* hf; bf16* qkvt; bf16* of; bf16* out; unsigned* cnt;
};
constexpr int WREG = 20224;
constexpr int KP_LDS = 163840;
constexpr int SYNC_OFF = 147456;

__global__ __launch_bounds__(512, 2) void kp_main(const KFArgs a) {
  extern __shared__ __attribute__((aligned(16))) char lds[];
  const int wave = __builtin_amdgcn_readfirstlane(threadIdx.x >> 6);
  const int w4 = wave & 3;
  const int b = blockIdx.x, xcd = b & 7, j = b >> 3;
  const unsigned ldsbase = (unsigned)(uintptr_t)(LAS char*)lds;
  const int t = xcd * 30 + j;
  const bool valid = j < 30 && t < 234;
  const int bmt = valid ? t / 9 : 0, nt = valid ? t % 9 : 0;
  const int mb0 = bmt * 5;
  const int nmb = min(5, 128 - mb0);
  char* cntb = (char*)a.cnt;
  const char* wdb = (const char*)a.wd + nt * 128;
  const unsigned ldsr = ldsbase + wave * 20480;
  if (threadIdx.x == 0) *(volatile LAS unsigned*)(uintptr_t)(ldsbase + KP_LDS - 16) = 0;
  STR(59);
  ST(0);
  {
    const int lane = fresh_lane();
    const int slot = xcd * 4 + (j & 3), mt = j >> 2;
    const int u0 = slot * 342 / 32, u1 = (slot + 1) * 342 / 32;
    const int nu1 = u1 - u0 - 7;
    const int g = lane >> 4, q = (lane & 15) >> 2, p = lane & 3;
    const unsigned rrow = 4 * g + q;
    const unsigned rdbase = ldsbase + rrow * 128 + (((rrow >> 1) & 3) << 5) + p * 8;
    const int rb = mt * 8 + wave, gmb = rb * 2;
    K1Ctx c;
    c.ldsw = ldsbase + wave * 1024;
    c.xb = (const char*)a.xnf + (size_t)gmb * 18 * 1024;
    c.dopre = __builtin_amdgcn_readfirstlane((valid && wave < 4) ? 1 : 0);
    c.prelds = ldsr;
    c.wdb = wdb; c.wd = (const char*)a.wd; c.wo = (const char*)a.wo; c.w4 = w4; c.tidx = b * 8 + wave; c.tlds = ldsbase + 147456 + wave * 256; c.trow = rb * 9;
    const int us[3] = {u0, u0 + 4, u0 + 4 + nu1};
    if (u0 < 288) {
      c.strideB = F_ * 2;
      c.flag = cntb + rb * 3 * 128;
      {
        const char* pg = (const char*)(a.wg + us[1] * 16); const char* pu = (const char*)(a.wu + us[1] * 16);
        const bool near = (pu >= pg ? (size_t)(pu - pg) : (size_t)(pg - pu)) < ((size_t)1 << 31);
        c.tbase = near ? (pu < pg ? pu : pg) : (wave & 1 ? pu : pg);
        c.tdelta = near ? (unsigned)(pu >= pg ? pu - pg : pg - pu) : 0u;
      }
      const bf16* wsel = wave < 4 ? a.wg : a.wu;
#pragma unroll
      for (int i = 0; i < 3; i++) {
        c.pW[i] = (const char*)(wsel + us[i] * 16);
        c.outp[i] = (char*)a.hf + (size_t)(gmb * 144 + (us[i] >> 1)) * 1024; c.upar[i] = us[i] & 1;
      }
      K1<true> k(c);
      k.rdbase = rdbase; k.xo = lane * 16; k.donev = ldsbase + KP_LDS - 16;
      k.voff4 = stage_voff(wave, lane, c.strideB, 4, 32); k.voff3 = stage_voff(wave, lane, c.strideB, 3, 32);
      k.voffm = nu1 == 4 ? k.voff4 : k.voff3;
      k.sto = (g & 1) * (144 * 1024) + ((g >> 1) * 16 + (lane & 15)) * 16;
      if (nu1 == 4) k.run<4>(); else k.run<3>();
    } else {
      c.strideB = QKVN * 2;
      c.flag = cntb;
      c.tbase = (const char*)(a.wqkv + (us[1] - 288) * 32); c.tdelta = 0;
#pragma unroll
      for (int i = 0; i < 3; i++) {
        c.pW[i] = (const char*)(a.wqkv + (us[i] - 288) * 32) + (wave < 4 ? 0 : 32);
        c.outp[i] = (char*)a.qkvt + (size_t)(gmb * 54 + (us[i] - 288)) * 1024; c.upar[i] = 0;
      }
      K1<false> k(c);
      k.rdbase = rdbase; k.xo = lane * 16; k.donev = ldsbase + KP_LDS - 16;
      k.voff4 = stage_voff(wave, lane, c.strideB, 4, 64); k.voff3 = stage_voff(wave, lane, c.strideB, 3, 64);
      k.voffm = nu1 == 4 ? k.voff4 : k.voff3;
      k.sto = lane * 16;
      if (nu1 == 4) k.run<4>(); else k.run<3>();
    }
    asm volatile("s_waitcnt vmcnt(0)" ::: "memory");
    publish(cntb + (u0 < 288 ? rb * 3 + 2 : 192 + rb) * 128);
    ST(32);
  }
  const int lane = fresh_lane();
  const int g = lane >> 4, q = (lane & 15) >> 2, p = lane & 3, m = lane & 15;
  char* ldsp = lds + wave * 20480;
  const unsigned scol = ((((lane & 7) >> 1) ^ ((lane >> 4) & 1)) << 5) + ((lane & 1) << 4);
  const unsigned voffw0 = (lane >> 3) * (D_ * 2) + scol;
  const unsigned voffw1 = (lane >> 3) * (D_ * 2) + (scol ^ 64);
  const unsigned rrow2 = 8 * g + q;
  const unsigned rdb = ldsr + rrow2 * 128 + ((((rrow2 >> 1) & 1) | ((g & 1) << 1)) << 5) + p * 8;
  f32x4 acc[5][4];
  auto zacc = [&]() {
#pragma unroll
    for (int i = 0; i < 5; i++)
#pragma unroll
      for (int c = 0; c < 4; c++) { acc[i][c] = f32x4{0, 0, 0, 0}; asm volatile("" : "+v"(acc[i][c])); }
    asm volatile("s_nop 3");
  };
  bf16x8 hb[4][5];
  auto wstage = [&](const char* wsrc, int sl) {
#pragma unroll
    for (int r = 0; r < 4; r++) glds16(wsrc + r * 8 * (D_ * 2), (r & 1) ? voffw1 : voffw0, ldsr + sl * 4096 + r * 1024);
  };
  auto compute = [&](bf16x8 (&src)[5], int sl) {
    s16x4 a0[4], a1[4];
#pragma unroll
    for (int c = 0; c < 4; c++) {
      unsigned ad = (rdb ^ (c << 5)) + sl * 4096;
      a0[c] = trread(ad); a1[c] = trread(ad + 512);
    }
    SB();
#pragma unroll
    for (int c = 0; c < 4; c++) {
      bf16x8 wa = cat8(a0[c], a1[c]);
#pragma unroll
      for (int i = 0; i < 5; i++) MFMA(acc[i][c], wa, src[i]);
    }
  };
#define WAITH(n, d) asm volatile("s_waitcnt vmcnt(%5)" : "+v"(hb[d][0]), "+v"(hb[d][1]), "+v"(hb[d][2]), "+v"(hb[d][3]), "+v"(hb[d][4]) : "i"(n) : "memory")
  auto attention = [&](const int w4) {
    const int mb = b >> 1, hh = b & 1;
    const int mbp = mb > 0 ? mb - 1 : 0;
    pollge(1, cntb + 192 * 128, (((lane & 1) ? mbp : mb) >> 1) * 128, 5u);
    const int c0 = 72 * (4 * hh + w4);
    const int v0 = c0 >> 5;
#pragma unroll
    for (int sec = 0; sec < 5; sec++) {
      const int smb = (sec == 1 || sec == 3) ? mbp : mb;
      const int ub = (sec == 0 ? 0 : (sec <= 2 ? 18 : 36));
#pragma unroll
      for (int v = 0; v < 3; v++)
        glds16_sc1((const char*)a.qkvt + (size_t)(smb * 54 + ub + v0 + v) * 1024, lane * 16, ldsr + (sec * 3 + v) * 1024);
    }
    int coff[9];
#pragma unroll
    for (int i = 0; i < 9; i++) {
      int cidx = c0 + g * DH + 2 * i - v0 * 32; int v = cidx >> 5, cc = cidx & 31;
      coff[i] = v * 1024 + ((cc >> 2) & 3) * 256 + (cc >> 4) * 8 + (cc & 3) * 2;
    }
    asm volatile("s_waitcnt vmcnt(0)" ::: "memory");
    unsigned qv[9];
#pragma unroll
    for (int i = 0; i < 9; i++) qv[i] = *(const unsigned*)(ldsp + coff[i] + m * 16);
    const int srow = mb * 16 + m;
    float lg[8]; float mx = -3.0e38f;
#pragma unroll
    for (int w = 0; w < 8; w++) {
      const int r = m + 16 - 2 * w;
      const char* kp = ldsp + 3072 + (r >> 4) * 3072 + (r & 15) * 16;
      float s_ = 0;
#pragma unroll
      for (int i = 0; i < 9; i++) s_ = dot2(qv[i], *(const unsigned*)(kp + coff[i]), s_);
      s_ = rbf(rbf(s_) * 0.23570226039551584f);
      s_ = (srow - 2 * w) >= 0 ? s_ : -1e9f;
      lg[w] = s_; mx = fmaxf(mx, s_);
    }
    float sum = 0;
#pragma unroll
    for (int w = 0; w < 8; w++) { lg[w] = __expf(lg[w] - mx); sum += lg[w]; }
    const float inv = 1.f / sum;
    float o[DH];
#pragma unroll
    for (int i = 0; i < DH; i++) o[i] = 0;
#pragma unroll
    for (int w = 0; w < 8; w++) {
      const int r = m + 16 - 2 * w;
      const char* vp = ldsp + 9216 + (r >> 4) * 3072 + (r & 15) * 16;
      const float pw = rbf(lg[w] * inv);
#pragma unroll
      for (int i = 0; i < 9; i++) { unsigned u = *(const unsigned*)(vp + coff[i]); o[2 * i] += pw * lo2f(u); o[2 * i + 1] += pw * hi2f(u); }
    }
#pragma unroll
    for (int i = 0; i < 9; i++) {
      const int cl = g * DH + 2 * i;
      *(unsigned*)(ldsp + ((cl >> 3) * 16 + m) * 16 + (cl & 7) * 2) = pk2(o[2 * i], o[2 * i + 1]);
    }
    asm volatile("s_waitcnt lgkmcnt(0)" ::: "memory");
    {
      char* ob = (char*)a.of + (size_t)mb * 18 * 1024 + (c0 >> 3) * 256;
      u32x4 v0_ = *(const u32x4*)(ldsp + lane * 16), v1_ = *(const u32x4*)(ldsp + 1024 + lane * 16), v2_ = *(const u32x4*)(ldsp + 2048 + (lane & 15) * 16);
      gst16_sc1(ob, lane * 16, v0_);
      gst16_sc1(ob + 1024, lane * 16, v1_);
      gst16_sc1_m(ob + 2048, lane * 16, v2_, 0xffffull);
    }
    asm volatile("s_waitcnt vmcnt(0) lgkmcnt(0)" ::: "memory");
    publish(cntb + (256 + mb) * 128);
  };

  const int i0 = (wave >> 2) ? 0 : 3, ni = (wave >> 2) ? 3 : 2;
  const int col = nt * 64 + w4 * 16 + 4 * g;
  u32x2 xv[3];
  auto xload = [&]() {
#pragma unroll
    for (int ii = 0; ii < 3; ii++) {
      const int row = min((mb0 + i0 + ii) * 16 + m, S_ - 1);
      xv[ii] = *(const u32x2*)((const char*)a.x + ((size_t)row * D_ + col) * 2);
    }
#pragma unroll
    for (int ii = 0; ii < 3; ii++) asm volatile("" : "+v"(xv[ii]));
  };

  if (wave < 4) {
    if (!valid) return;
    unsigned voffh[5];
#pragma unroll
    for (int i = 0; i < 5; i++) voffh[i] = (mb0 + min(i, nmb - 1)) * 144 * 1024 + lane * 16;
    const unsigned voffc = ((mb0 + min(lane, nmb - 1)) >> 1) * (3 * 128);
    unsigned tab = kOrder[min(4 * lane + w4, 143)];
    xload();
    asm volatile("" : "+v"(tab));
    int ready = -1;
    auto hload = [&](int it, bf16x8 (&dst)[5]) {
      const unsigned ent = __builtin_amdgcn_readlane(tab, it);
      const int tau = ent >> 8, ks = ent & 255;
      pollge(tau - ready, cntb + tau * 128, voffc, 27u);
      ready = tau;
      const char* hsrc = (const char*)a.hf + (size_t)ks * 1024;
#pragma unroll
      for (int i = 0; i < 5; i++) gld16_sc1(dst[i], hsrc, voffh[i]);
    };
    auto body = [&](bf16x8 (&src)[5], int sl, int itn) {
      compute(src, sl);
      const unsigned ent = __builtin_amdgcn_readlane(tab, itn);
      wstage(wdb + (size_t)(ent & 255) * 32 * (D_ * 2), sl);
      hload(itn, src);
    };
    hload(0, hb[0]); hload(1, hb[1]); hload(2, hb[2]);
    zacc();
    ST(33);
    WAITH(10, 0); body(hb[0], 0, 3);
    WAITH(14, 1); body(hb[1], 1, 4);
    WAITH(18, 2); body(hb[2], 2, 5);
#pragma unroll 1
    for (int it = 3; it < 33; it += 3) {
      WAITH(18, 0); body(hb[0], 0, it + 3);
      WAITH(18, 1); body(hb[1], 1, it + 4);
      WAITH(18, 2); body(hb[2], 2, it + 5);
    }
    WAITH(18, 0); compute(hb[0], 0);
    WAITH(9, 1); compute(hb[1], 1);
    WAITH(0, 2); compute(hb[2], 2);
    ST(34);
  } else {
    __builtin_amdgcn_s_setprio(2);
    lds_wait<0>(ldsbase + KP_LDS - 16, 8);
    ST(33);
    attention(w4);
    ST(34);
    if (!valid) return;
    xload();
    const char* wsrc = (const char*)a.wo + nt * 128 + (size_t)w4 * 32 * (D_ * 2);
    const int l5 = w4 < 2 ? 4 : 0;
#pragma unroll
    for (int i = 0; i < 4; i++) wstage(wsrc + (size_t)i * 4 * 32 * (D_ * 2), i);
    zacc();
    pollge(1, cntb + 256 * 128, (mb0 + min(lane, nmb - 1)) * 128, 8u);
    ST(35);
    unsigned voffo[5];
#pragma unroll
    for (int i = 0; i < 5; i++) voffo[i] = (mb0 + min(i, nmb - 1)) * 18 * 1024 + lane * 16;
    const char* osrc = (const char*)a.of + w4 * 1024;
#pragma unroll
    for (int d = 0; d < 4; d++)
#pragma unroll
      for (int i = 0; i < 5; i++) gld16_sc1(hb[d][i], osrc + d * 4096, voffo[i]);
    WAITH(15, 0); compute(hb[0], 0);
    wstage(wsrc + (size_t)l5 * 4 * 32 * (D_ * 2), 0);
#pragma unroll
    for (int i = 0; i < 5; i++) gld16_sc1(hb[0][i], osrc + l5 * 4096, voffo[i]);
    WAITH(19, 1); compute(hb[1], 1);
    WAITH(14, 2); compute(hb[2], 2);
    WAITH(9, 3); compute(hb[3], 3);
    WAITH(0, 0);
    if (w4 < 2) compute(hb[0], 0);
    ST(36);
  }
  MFMA_DRAIN();
#pragma unroll
  for (int i = 0; i < 5; i++)
#pragma unroll
    for (int c = 0; c < 4; c++) *(f32x4*)(ldsp + ((c * 5 + i) * 64 + lane) * 16) = acc[i][c];
  __syncthreads();
  ST(37);
#pragma unroll
  for (int ii = 0; ii < 3; ii++) {
    const int i = i0 + ii;
    if (ii < ni && i < nmb) {
      const char* pp = lds + ((w4 * 5 + i) * 64 + lane) * 16;
      f32x4 d4 = (*(const f32x4*)(pp) + *(const f32x4*)(pp + 20480)) + (*(const f32x4*)(pp + 2 * 20480) + *(const f32x4*)(pp + 3 * 20480));
      f32x4 s4 = (*(const f32x4*)(pp + 4 * 20480) + *(const f32x4*)(pp + 5 * 20480)) + (*(const f32x4*)(pp + 6 * 20480) + *(const f32x4*)(pp + 7 * 20480));
      const float xs[4] = {lo2f(xv[ii][0]), hi2f(xv[ii][0]), lo2f(xv[ii][1]), hi2f(xv[ii][1])};
      float o[4];
#pragma unroll
      for (int e = 0; e < 4; e++) {
        float d = rbf(d4[e]);
        float sg = rbf(d * __builtin_amdgcn_rcpf(1.f + __expf(-d)));
        o[e] = xs[e] + rbf(rbf(s4[e]) * sg);
      }
      u32x2 r; r[0] = pk2(o[0], o[1]); r[1] = pk2(o[2], o[3]);
      const int row = (mb0 + i) * 16 + m;
      *(u32x2*)((char*)a.out + ((size_t)row * D_ + col) * 2) = r;
    }
  }
  ST(62);
  ST(63);
  STR(60);
}

extern "C" void kernel_launch(void* const* d_in, const int* in_sizes, int n_in, void* d_out, int out_size, void* d_ws,
                              size_t ws_size, hipStream_t stream) {
  KFArgs a;
  a.x = (const bf16*)d_in[0];
  a.wqkv = (const bf16*)d_in[1];
  a.wo = (const bf16*)d_in[2];
  a.wg = (const bf16*)d_in[4];
  a.wu = (const bf16*)d_in[5];
  a.wd = (const bf16*)d_in[6];
  a.out = (bf16*)d_out;
  char* ws = (char*)d_ws;
  bf16* xnf = (bf16*)ws; ws += (size_t)S_ * D_ * 2;
  a.xnf = xnf;
  a.of = (bf16*)ws; ws += (size_t)S_ * D_ * 2;
  a.qkvt = (bf16*)ws; ws += (size_t)128 * 54 * 1024;
  a.hf = (bf16*)ws; ws += (size_t)S_ * F_ * 2;
  a.cnt = (unsigned*)ws; ws += 384 * 128;
  (void)hipFuncSetAttribute((const void*)kp_main, hipFuncAttributeMaxDynamicSharedMemorySize, KP_LDS);
  k0_ln<<<256, 256, 0, stream>>>(a.x, xnf, a.cnt, a.wg, a.wu, a.wqkv);
  kp_main<<<256, 512, KP_LDS, stream>>>(a);
}
```

```cpp
#include <hip/hip_runtime.h>
#include <hip/hip_bf16.h>
#include <stdint.h>

typedef __hip_bfloat16 bf16;
typedef short bf16x8 __attribute__((ext_vector_type(8)));
typedef short s16x4 __attribute__((ext_vector_type(4)));
typedef float f32x4 __attribute__((ext_vector_type(4)));
typedef float f32x2 __attribute__((ext_vector_type(2)));
typedef __bf16 bf2 __attribute__((ext_vector_type(2)));
typedef unsigned u32x2 __attribute__((ext_vector_type(2)));
typedef unsigned u32x4 __attribute__((ext_vector_type(4)));
#define LAS __attribute__((address_space(3)))
#define DI __device__ __forceinline__

constexpr int PRO = 18;
constexpr int TPOS = 23;
constexpr int W1POS = 49;
constexpr int CODE_LINES = 760;
constexpr int S_ = 2048, D_ = 576, F_ = 4608, DH = 18, QKVN = 1728;

DI unsigned pk2(float a, float b) { f32x2 v = {a, b}; bf2 r = __builtin_convertvector(v, bf2); return __builtin_bit_cast(unsigned, r); }
DI float rbf(float a) { return __uint_as_float(pk2(a, 0.f) << 16); }
DI float lo2f(unsigned u) { return __uint_as_float(u << 16); }
DI float hi2f(unsigned u) { return __uint_as_float(u & 0xffff0000u); }

__global__ __launch_bounds__(256) void k0_ln(const bf16* __restrict__ x, bf16* __restrict__ xnf, unsigned* __restrict__ cnt,
                                             const bf16* __restrict__ wg, const bf16* __restrict__ wu, const bf16* __restrict__ wqkv) {
  const int tid = threadIdx.x;
  if (blockIdx.x >= 128) {
    const int pb = blockIdx.x - 128, xcd = pb & 7, jb = pb >> 3;
    constexpr int NL = 3, RS = 16;
    const int r0 = jb * 36 + (tid >> 4), sl = xcd * 4 + ((tid >> 2) & 3), e = tid & 3;
    const char* base; unsigned stride;
    if (sl < 27) { base = (const char*)((e & 1) ? wu : wg) + sl * 256; stride = F_ * 2; }
    else { base = (const char*)wqkv + (sl - 27) * 512 + (e & 1) * 128; stride = QKVN * 2; }
    unsigned v[NL];
#pragma unroll
    for (int i = 0; i < NL; i++) v[i] = *(const unsigned*)(base + (size_t)min(r0 + i * RS, jb * 36 + 35) * stride);
    unsigned acc = 0;
#pragma unroll
    for (int i = 0; i < NL; i++) acc ^= v[i];
    {
      unsigned clo, chi;
      asm volatile("s_getpc_b64 s[20:21]\n\ts_add_u32 %0, s20, _Z7kp_main6KFArgs@rel32@lo+4\n\ts_addc_u32 %1, s21, _Z7kp_main6KFArgs@rel32@hi+12"
                   : "=s"(clo), "=s"(chi) :: "scc", "s20", "s21");
      const __attribute__((address_space(1))) char* code = (const __attribute__((address_space(1))) char*)(((unsigned long long)chi << 32) | clo);
      const int ci = jb * 256 + tid;
      if (ci < CODE_LINES) acc ^= *(const __attribute__((address_space(1))) unsigned*)(code + ci * 128);
    }
    asm volatile("" :: "v"(acc));
    return;
  }
  if (tid < 4) cnt[(blockIdx.x * 4 + tid) * 32] = 0;
  const int row = blockIdx.x * 16 + (tid >> 4), c = tid & 15;
  const char* xr = (const char*)x + (size_t)row * (D_ * 2) + c * 8;
  u32x2 v[9];
#pragma unroll
  for (int i = 0; i < 9; i++) v[i] = *(const u32x2*)(xr + i * 128);
  float f[36];
#pragma unroll
  for (int i = 0; i < 9; i++) { f[4 * i] = lo2f(v[i][0]); f[4 * i + 1] = hi2f(v[i][0]); f[4 * i + 2] = lo2f(v[i][1]); f[4 * i + 3] = hi2f(v[i][1]); }
  float s = 0;
#pragma unroll
  for (int i = 0; i < 36; i++) s += f[i];
  s += __shfl_xor(s, 1); s += __shfl_xor(s, 2); s += __shfl_xor(s, 4); s += __shfl_xor(s, 8);
  const float mean = s * (1.f / D_);
  float q = 0;
#pragma unroll
  for (int i = 0; i < 36; i++) { float d = f[i] - mean; q += d * d; }
  q += __shfl_xor(q, 1); q += __shfl_xor(q, 2); q += __shfl_xor(q, 4); q += __shfl_xor(q, 8);
  const float mb16 = rbf(mean);
  const float var = rbf(q * (1.f / D_));
  const float den = rbf(sqrtf(rbf(var + 1e-5f)));
  const float inv = 1.f / den;
  char* ob = (char*)xnf + (size_t)blockIdx.x * 18 * 1024 + (c >> 3) * 1024 + ((c & 3) * 16 + (row & 15)) * 16 + ((c >> 2) & 1) * 8;
#pragma unroll
  for (int i = 0; i < 9; i++) {
    u32x2 r;
    r[0] = pk2(rbf(f[4 * i] - mb16) * inv, rbf(f[4 * i + 1] - mb16) * inv);
    r[1] = pk2(rbf(f[4 * i + 2] - mb16) * inv, rbf(f[4 * i + 3] - mb16) * inv);
    *(u32x2*)(ob + i * 2048) = r;
  }
}

DI void glds16(const char* sbase, unsigned voff, unsigned ldsaddr) {
  asm volatile("s_mov_b32 m0, %0\n\ts_nop 0\n\tglobal_load_lds_dwordx4 %1, %2" ::"s"(ldsaddr), "v"(voff), "s"(sbase) : "memory");
}
DI void glds16_sc1(const char* sbase, unsigned voff, unsigned ldsaddr) {
  asm volatile("s_mov_b32 m0, %0\n\ts_nop 0\n\tglobal_load_lds_dwordx4 %1, %2 sc1" ::"s"(ldsaddr), "v"(voff), "s"(sbase) : "memory");
}
DI void gld16(bf16x8& dst, const char* sbase, unsigned voff) {
  asm volatile("global_load_dwordx4 %0, %1, %2" : "=v"(dst) : "v"(voff), "s"(sbase) : "memory");
}
DI void gld16_sc1(bf16x8& dst, const char* sbase, unsigned voff) {
  asm volatile("global_load_dwordx4 %0, %1, %2 sc1" : "=v"(dst) : "v"(voff), "s"(sbase) : "memory");
}
DI void gst16_sc1(char* sbase, unsigned voff, u32x4 v) {
  asm volatile("global_store_dwordx4 %0, %1, %2 sc1\n\ts_nop 1" :: "v"(voff), "v"(v), "s"(sbase) : "memory");
}
DI void gst16_sc1_m(char* sbase, unsigned voff, u32x4 v, unsigned long long mask) {
  asm volatile("s_mov_b64 exec, %3\n\tglobal_store_dwordx4 %0, %1, %2 sc1\n\ts_mov_b64 exec, -1\n\ts_nop 0" :: "v"(voff), "v"(v), "s"(sbase), "s"(mask) : "memory");
}
DI s16x4 trread(unsigned addr) { return __builtin_amdgcn_ds_read_tr16_b64_v4i16((LAS s16x4*)(uintptr_t)addr); }
DI bf16x8 cat8(s16x4 a, s16x4 b) { return __builtin_shufflevector(a, b, 0, 1, 2, 3, 4, 5, 6, 7); }
DI float dot2(unsigned a, unsigned b, float c) { return __builtin_amdgcn_fdot2_f32_bf16(__builtin_bit_cast(bf2, a), __builtin_bit_cast(bf2, b), c, false); }
DI int fresh_lane() {
  int l;
  asm volatile("v_mbcnt_lo_u32_b32 %0, -1, 0\n\tv_mbcnt_hi_u32_b32 %0, -1, %0" : "=v"(l));
  return l;
}
#define SB() __builtin_amdgcn_sched_barrier(0)
#define MFMA(c, a, b) asm volatile("v_mfma_f32_16x16x32_bf16 %0, %1, %2, %0" : "+v"(c) : "v"(a), "v"(b))
#define MFMA0(c, a, b) asm volatile("v_mfma_f32_16x16x32_bf16 %0, %1, %2, 0" : "=&v"(c) : "v"(a), "v"(b))
#define VMUL(d, a, b) asm volatile("v_mul_f32 %0, %1, %2" : "=v"(d) : "v"(a), "v"(b))
#define MFMA_DRAIN() do { asm volatile("s_nop 15\n\ts_nop 7" ::: "memory"); SB(); } while (0)

DI void lds_add1(unsigned vaddr) {
  unsigned t;
  asm volatile("s_mov_b64 exec, 1\n\tv_mov_b32 %0, 1\n\tds_add_u32 %1, %0\n\ts_mov_b64 exec, -1" : "=&v"(t) : "v"(vaddr) : "memory");
}
template <int OFF> DI void lds_wait(unsigned vaddr, unsigned target) {
  unsigned t; unsigned sc, sp;
  asm volatile(
      "s_mov_b32 %2, 0\n"
      "Lsb%=:\n\t"
      "ds_read_b32 %0, %3 offset:%5\n\t"
      "s_add_u32 %2, %2, 1\n\t"
      "s_cmp_gt_u32 %2, 0x100000\n\t"
      "s_waitcnt lgkmcnt(0)\n\t"
      "s_cbranch_scc1 Lse%=\n\t"
      "v_readfirstlane_b32 %1, %0\n\t"
      "s_cmp_lt_u32 %1, %4\n\t"
      "s_cbranch_scc1 Lsb%=\n"
      "Lse%=:\n\t"
      : "=&v"(t), "=&s"(sc), "=&s"(sp) : "v"(vaddr), "s"(target), "i"(OFF) : "memory", "scc");
}
template <int OFF> DI void lds_wait_if(unsigned have, unsigned vaddr, unsigned target) {
  unsigned t; unsigned sc, sp;
  asm volatile(
      "s_cmp_ge_u32 %6, %4\n\t"
      "s_cbranch_scc1 Lse%=\n\t"
      "s_mov_b32 %2, 0\n"
      "Lsb%=:\n\t"
      "ds_read_b32 %0, %3 offset:%5\n\t"
      "s_add_u32 %2, %2, 1\n\t"
      "s_cmp_gt_u32 %2, 0x100000\n\t"
      "s_waitcnt lgkmcnt(0)\n\t"
      "s_cbranch_scc1 Lse%=\n\t"
      "v_readfirstlane_b32 %1, %0\n\t"
      "s_cmp_lt_u32 %1, %4\n\t"
      "s_cbranch_scc1 Lsb%=\n"
      "Lse%=:\n\t"
      : "=&v"(t), "=&s"(sc), "=&s"(sp) : "v"(vaddr), "s"(target), "i"(OFF), "s"(have) : "memory", "scc");
}
template <int OFF> DI void lds_wait_slow(unsigned vaddr, unsigned target) {
  unsigned t; unsigned sc, sp;
  asm volatile(
      "s_mov_b32 %2, 0\n"
      "Lsb%=:\n\t"
      "ds_read_b32 %0, %3 offset:%5\n\t"
      "s_add_u32 %2, %2, 1\n\t"
      "s_cmp_gt_u32 %2, 0x40000\n\t"
      "s_waitcnt lgkmcnt(0)\n\t"
      "s_cbranch_scc1 Lse%=\n\t"
      "v_readfirstlane_b32 %1, %0\n\t"
      "s_cmp_ge_u32 %1, %4\n\t"
      "s_cbranch_scc1 Lse%=\n\t"
      "s_sleep 4\n\t"
      "s_branch Lsb%=\n"
      "Lse%=:\n\t"
      : "=&v"(t), "=&s"(sc), "=&s"(sp) : "v"(vaddr), "s"(target), "i"(OFF) : "memory", "scc");
}
DI void publish(char* sbase) {
  unsigned z, o;
  asm volatile(
      "s_mov_b64 exec, 1\n\t"
      "v_mov_b32 %0, 0\n\t"
      "v_mov_b32 %1, 1\n\t"
      "global_atomic_add %0, %1, %2\n\t"
      "s_mov_b64 exec, -1\n\t"
      : "=&v"(z), "=&v"(o) : "s"(sbase) : "memory");
}
DI void pollge(int need, const char* sbase, unsigned voff, unsigned target) {
  unsigned t; unsigned sp;
  asm volatile(
      "s_cmp_eq_u32 %2, 0\n\t"
      "s_cbranch_scc1 Lpe%=\n\t"
      "s_mov_b32 %1, 0\n"
      "Lpl%=:\n\t"
      "global_load_dword %0, %3, %4 sc1\n\t"
      "s_waitcnt vmcnt(0)\n\t"
      "v_cmp_gt_u32 vcc, %5, %0\n\t"
      "s_cbranch_vccz Lpa%=\n\t"
      "s_sleep 8\n\t"
      "s_add_u32 %1, %1, 1\n\t"
      "s_cmp_lt_u32 %1, 0x20000\n\t"
      "s_cbranch_scc1 Lpl%=\n"
      "Lpa%=:\n\t"
      "Lpe%=:\n\t"
      : "=&v"(t), "=&s"(sp) : "s"(need), "v"(voff), "s"(sbase), "s"(target) : "memory", "scc", "vcc");
}

DI void pollge_slow(const char* sbase, unsigned voff, unsigned target) {
  unsigned t; unsigned sp;
  asm volatile(
      "s_mov_b32 %1, 0\n"
      "Lpl%=:\n\t"
      "global_load_dword %0, %2, %3 sc1\n\t"
      "s_waitcnt vmcnt(0)\n\t"
      "v_cmp_gt_u32 vcc, %4, %0\n\t"
      "s_cbranch_vccz Lpe%=\n\t"
      "s_sleep 24\n\t"
      "s_add_u32 %1, %1, 1\n\t"
      "s_cmp_lt_u32 %1, 0x10000\n\t"
      "s_cbranch_scc1 Lpl%=\n"
      "Lpe%=:\n\t"
      : "=&v"(t), "=&s"(sp) : "v"(voff), "s"(sbase), "s"(target) : "memory", "scc", "vcc");
}
DI void glds16_if(int on, const char* sbase, unsigned voff, unsigned ldsaddr) {
  asm volatile("s_cmp_eq_u32 %3, 0\n\ts_cbranch_scc1 Lg%=\n\ts_mov_b32 m0, %0\n\ts_nop 0\n\tglobal_load_lds_dwordx4 %1, %2\nLg%=:" ::"s"(ldsaddr), "v"(voff), "s"(sbase), "s"(on) : "memory", "scc");
}
struct K1Ctx {
  const char* pW[3];
  char* outp[3];
  const char* xb;
  unsigned ldsw, strideB;
  int upar[3];
  char* flag;
  const char* wdb; const char* wd; const char* wo; int w4, tidx, trow, xidx; unsigned tlds, tdelta, tq;
  unsigned prelds;
  int dopre;
};
constexpr int KS_B = 8192;
struct WF { s16x4 a0, a1, b0, b1; };

template <bool FFN>
struct K1 {
  const K1Ctx& c;
  bf16x8 xf[18][2];
  f32x4 acc[4][2][2];
  WF wf[3];
  unsigned pk[4];
  unsigned rdbase, voff4, voff3, voffm, xo, sto, ra, donev;
  float t0, t1, t2, t3;
  DI K1(const K1Ctx& c_) : c(c_) {}

  template <int P, int S> DI void stage() {
    const unsigned vo = P <= 1 ? voff4 : voff3;
    glds16(c.pW[P] + (size_t)S * 32 * c.strideB, vo, c.ldsw + S * KS_B);
  }
  template <int S, int MB> DI void xload() { gld16(xf[S][MB], c.xb + (MB * 18 + S) * 1024, xo); }
  template <int K> DI void xwload() {
    constexpr int S = K / 3, r = K % 3;
    if constexpr (r < 2) xload<S, r>(); else stage<0, S>();
  }
  template <int NU1> DI void touch12() {
    const int l = fresh_lane();
    const int r = c.trow + min(l / 3, 17), e = l % 3;
    const unsigned d2 = (unsigned)(c.pW[2] - c.pW[1]);
    unsigned off;
    if constexpr (FFN) off = r * c.strideB + (e == 0 ? 0u : d2 + (e == 1 ? 0u : c.tdelta));
    else off = r * c.strideB + (e == 0 ? c.tq : d2 + (e == 1 ? 0u : c.tdelta));
    asm volatile("s_mov_b32 m0, %0\n\ts_nop 0\n\tglobal_load_lds_dword %1, %2" ::"s"(c.tlds), "v"(off), "s"(c.pW[1]) : "memory");
  }
  static constexpr int EPI_N = 45;
  template <int P, int U, int M> DI void epi() {
    if constexpr (FFN) {
      const float c1 = 2.3022081985f, c2 = 0.1029432397f;
      constexpr int E2 = M / 21, r = M % 21;
      if constexpr (E2 < 2) {
        constexpr int sub = r <= 10 ? 0 : 1;
        constexpr int ph = sub == 0 ? r : r - 10;
        constexpr int E = E2 * 2 + sub;
        constexpr int mb = E >> 1, h = (E & 1) * 2;
        const float ga = acc[U][0][mb][h], gb = acc[U][0][mb][h + 1], ua = acc[U][1][mb][h], ub = acc[U][1][mb][h + 1];
        if constexpr (sub == 0) {
          if constexpr (ph == 0) { VMUL(t0, ga, ga); VMUL(t1, gb, gb); }
          if constexpr (ph == 1) { t0 = __builtin_fmaf(t0, -c2, -c1); t1 = __builtin_fmaf(t1, -c2, -c1); }
          if constexpr (ph == 2) { VMUL(t0, ga, t0); VMUL(t1, gb, t1); }
          if constexpr (ph == 3) { t0 = __builtin_amdgcn_exp2f(t0); }
          if constexpr (ph == 4) { t1 = __builtin_amdgcn_exp2f(t1); }
          if constexpr (ph == 5) { t0 = 1.f + t0; asm volatile("" : "+v"(t0)); t1 = 1.f + t1; }
          if constexpr (ph == 6) { t0 = __builtin_amdgcn_rcpf(t0); }
          if constexpr (ph == 7) { t1 = __builtin_amdgcn_rcpf(t1); }
          if constexpr (ph == 8) { VMUL(t2, ga, ua); VMUL(t3, gb, ub); }
          if constexpr (ph == 9) { VMUL(t2, t2, t0); VMUL(t3, t3, t1); }
          if constexpr (ph == 10) {
            pk[E] = pk2(t2, t3);
            const float gn = acc[U][0][mb][2];
            VMUL(t0, gn, gn);
          }
        } else {
          if constexpr (ph == 1) { VMUL(t1, gb, gb); t0 = __builtin_fmaf(t0, -c2, -c1); }
          if constexpr (ph == 2) { t1 = __builtin_fmaf(t1, -c2, -c1); VMUL(t0, ga, t0); }
          if constexpr (ph == 3) { t0 = __builtin_amdgcn_exp2f(t0); }
          if constexpr (ph == 4) { VMUL(t1, gb, t1); t0 = 1.f + t0; }
          if constexpr (ph == 5) { t1 = __builtin_amdgcn_exp2f(t1); }
          if constexpr (ph == 6) { t0 = __builtin_amdgcn_rcpf(t0); }
          if constexpr (ph == 7) { t1 = 1.f + t1; VMUL(t2, ga, ua); }
          if constexpr (ph == 8) { t1 = __builtin_amdgcn_rcpf(t1); }
          if constexpr (ph == 9) { VMUL(t3, gb, ub); VMUL(t2, t2, t0); }
          if constexpr (ph == 10) { VMUL(t3, t3, t1); pk[E] = pk2(t2, t3); }
        }
        asm volatile("" : "+v"(t0), "+v"(t1), "+v"(t2), "+v"(t3));
      } else if constexpr (r == 1) {
        auto r0 = __builtin_amdgcn_permlane16_swap(pk[0], pk[2], false, false);
        auto r1 = __builtin_amdgcn_permlane16_swap(pk[1], pk[3], false, false);
        pk[0] = r0[0]; pk[2] = r0[1]; pk[1] = r1[0]; pk[3] = r1[1];
        asm volatile("" : "+v"(pk[0]), "+v"(pk[1]), "+v"(pk[2]), "+v"(pk[3]));
      } else if constexpr (r == 2) {
        u32x4 v = {pk[0], pk[1], pk[2], pk[3]};
        const int Ug = c.upar[P] + U;
        gst16_sc1(c.outp[P] + (Ug >> 1) * 1024 + (Ug & 1) * 512, sto, v);
      }
    } else {
      if constexpr (M < 10) {
        constexpr int mb = M / 5, r = M % 5;
        if constexpr (r < 2) { pk[2 * r] = pk2(acc[U][r][mb][0], acc[U][r][mb][1]); pk[2 * r + 1] = pk2(acc[U][r][mb][2], acc[U][r][mb][3]); asm volatile("" : "+v"(pk[2 * r]), "+v"(pk[2 * r + 1])); }
        if constexpr (r == 3) { u32x4 v = {pk[0], pk[1], pk[2], pk[3]}; gst16_sc1(c.outp[P] + mb * (54 * 1024) + U * 1024, sto, v); }
      }
    }
  }
  template <int P, int NU> static constexpr int epi_start(int U) {
    if (P == 0) return U == 0 ? 112 : 106 + 36 * U;
    int t = 0;
    for (int i = 0; i <= U; i++) { int av = 4 * (9 * (i + 1) + 1); t = (i == 0) ? av : (t + EPI_N > av ? t + EPI_N : av); }
    return t;
  }
  template <int P> static constexpr int epi_span(int U) { return P == 0 ? (U == 0 ? 30 : 36) : EPI_N; }
  template <int P, int NU> static constexpr int epi_t(int U, int M) { return epi_start<P, NU>(U) + (M * epi_span<P>(U)) / EPI_N; }
  template <int P, int NU> static constexpr int nst(int T0, int T1) {
    int n = 0;
    for (int U = 0; U < NU; U++) {
      if (FFN) { int t = epi_t<P, NU>(U, 44); n += (t >= T0 && t < T1); }
      else { int t = epi_t<P, NU>(U, 3); n += (t >= T0 && t < T1); t = epi_t<P, NU>(U, 8); n += (t >= T0 && t < T1); }
    }
    return n;
  }
  template <int P, int NU, int T, int U, int M> DI void epi_ms() {
    if constexpr (M < EPI_N) {
      if constexpr (epi_t<P, NU>(U, M) == T) epi<P, U, M>();
      if constexpr (epi_t<P, NU>(U, M) <= T) epi_ms<P, NU, T, U, M + 1>();
    }
  }
  template <int P, int NU, int T, int U = 0> DI void epi_at() {
    if constexpr (U < NU) {
      if constexpr (T >= epi_start<P, NU>(U) && T < epi_start<P, NU>(U) + epi_span<P>(U)) epi_ms<P, NU, T, U, 0>();
      epi_at<P, NU, T, U + 1>();
    }
  }
  template <int P, int NU, int T0, int T1> DI void epi_range() {
    if constexpr (T0 < T1) { epi_at<P, NU, T0>(); epi_range<P, NU, T0 + 1, T1>(); }
  }
  template <int P, int NU> static constexpr int st_u(int n) { return (P == 0) ? (n % (3 * NU)) / 3 : (n % (9 * NU)) / 9; }
  template <int P, int NU> static constexpr int st_s(int n) { return (P == 0) ? (n / (3 * NU)) * 3 + n % 3 : (n / (9 * NU)) * 9 + n % 9; }
  template <int P, int NU> static constexpr bool st_b(int n) { return (P == 0) ? n % (3 * NU) == 0 : n % (9 * NU) == 0; }

  static constexpr int nis(int m) { int n = PRO + (m / 12) * 9 + ((m % 12) < 9 ? (m % 12) : 9); return n > 54 ? 54 : n; }
  template <int K0, int K1> DI void xwrange() { if constexpr (K0 < K1) { xwload<K0>(); xwrange<K0 + 1, K1>(); } }
  template <int S0, int NS, int W> DI void waitx() {
    if constexpr (NS == 3) {
      asm volatile("s_waitcnt vmcnt(%6) lgkmcnt(0)"
                   : "+v"(xf[S0][0]), "+v"(xf[S0][1]), "+v"(xf[S0 + 1][0]), "+v"(xf[S0 + 1][1]), "+v"(xf[S0 + 2][0]), "+v"(xf[S0 + 2][1])
                   : "i"(W) : "memory");
    } else { waitx<S0, 3, W>(); waitx<S0 + 3, 3, W>(); waitx<S0 + 6, 3, W>(); }
  }

  template <int P, int NU, int NUP, int NUN, int N> DI void step() {
    constexpr int NST = 18 * NU, H = 9 * NU;
    constexpr int n2 = N + 2;
    constexpr bool nextp = n2 >= NST;
    constexpr bool rd = !nextp || NUN > 0;
    constexpr int NU2 = nextp ? (NUN > 0 ? NUN : 1) : NU;
    constexpr int m2 = nextp ? n2 - NST : n2;
    constexpr int P2 = nextp ? P + 1 : P;
    constexpr int u2 = st_u<P2, NU2>(m2), s2 = st_s<P2, NU2>(m2);
    constexpr bool bnd = rd && st_b<P2, NU2>(m2);
    constexpr int h2 = m2 / (9 * NU2), pos = m2 % (9 * NU2);
    constexpr int reg2 = s2 / 6, ro = (s2 % 6) * KS_B;
    constexpr bool newra = rd && (m2 == 0 || st_u<P2, NU2>(m2 - 1) != u2 || st_s<P2, NU2>(m2 - 1) / 6 != reg2);
    constexpr int NN = N < 0 ? 0 : N;
    constexpr int u = st_u<P, NU>(NN), s = st_s<P, NU>(NN);
    WF& wn = wf[n2 % 3];
    const WF& w = wf[NN % 3];
    bf16x8 wa = __builtin_shufflevector(w.a0, w.a1, 0, 1, 2, 3, 4, 5, 6, 7);
    bf16x8 wb = __builtin_shufflevector(w.b0, w.b1, 0, 1, 2, 3, 4, 5, 6, 7);
    if constexpr (newra) { asm volatile("v_xor_b32 %0, %2, %1\n\tv_add_u32 %0, %3, %0" : "=&v"(ra) : "v"(rdbase), "i"(u2 << 5), "i"(reg2 * 6 * KS_B)); }
#define GAP(K, ACC, WW, XX, RD)                                                                   \
    if constexpr (N >= 0) { if constexpr (s == 0) MFMA0(ACC, WW, XX); else MFMA(ACC, WW, XX); }   \
    SB();                                                                                         \
    if constexpr (rd && !bnd) { RD; }                                                             \
    if constexpr (N >= 0 && NUP > 0 && N < H + 4) epi_at<P - 1, NUP, 4 * (9 * NUP + N) + K>();    \
    if constexpr (N >= H) epi_at<P, NU, 4 * (N - H) + K>();                                       \
    SB();
    GAP(0, acc[u][0][0], wa, xf[s][0], wn.a0 = trread(ra + ro))
    GAP(1, acc[u][0][1], wa, xf[s][1], wn.a1 = trread(ra + ro + 2048))
    if constexpr (rd) {
      if constexpr (P2 == 0) {
        if constexpr (m2 >= 1) xwrange<nis(m2 - 1), nis(m2)>();
        if constexpr (m2 == TPOS) touch12<NUN>();
        if constexpr (m2 >= W1POS && m2 < W1POS + 9) stage<1, m2 - W1POS>();
      } else if constexpr (pos >= 1 && pos <= 17 && (pos & 1) == 1) {
        constexpr int TP = h2 == 0 ? P2 : P2 + 1, TS = (h2 == 0 ? 9 : 0) + (pos >> 1);
        if constexpr (TP <= 2) stage<TP, TS>();
      }
      if constexpr (P2 == 2 && h2 == 1 && pos >= 1 && pos <= 12) {
        constexpr int k = pos - 1;
        glds16_if(c.dopre, c.wdb + (size_t)(c.w4 + 4 * (k >> 2)) * 32 * (D_ * 2) + (k & 3) * 8 * (D_ * 2), (k & 1) ? voffm : voff4, c.prelds + k * 1024);
      }
      if constexpr (P2 == 1 && h2 == 0 && pos == 20) {
        const int l = fresh_lane();
        const unsigned off = (unsigned)min(c.tidx * 21 + min(l, 20), 41471) * 128u;
        asm volatile("s_mov_b32 m0, %0\n\ts_nop 0\n\tglobal_load_lds_dword %1, %2" ::"s"(c.tlds), "v"(off), "s"(c.wd) : "memory");
        const unsigned off2 = (unsigned)min(c.tidx * 3 + min(l, 2), 5183) * 128u;
        asm volatile("s_mov_b32 m0, %0\n\ts_nop 0\n\tglobal_load_lds_dword %1, %2" ::"s"(c.tlds), "v"(off2), "s"(c.wo) : "memory");
      }
      if constexpr (P2 == 2 && h2 == 0 && pos == 14) {
        const int l = fresh_lane();
        const unsigned scol = ((((l & 7) >> 1) ^ ((l >> 4) & 1)) << 5) + ((l & 1) << 4);
        voff4 = (l >> 3) * (D_ * 2) + scol; voffm = (l >> 3) * (D_ * 2) + (scol ^ 64);
      }
    }
    if constexpr (FFN && P == 2 && NU == 2 && N == H + 14) { asm volatile("s_waitcnt vmcnt(0)" ::: "memory"); publish(c.flag + 128); }
    GAP(2, acc[u][1][0], wb, xf[s][0], wn.b0 = trread(ra + ro + 4096))
    GAP(3, acc[u][1][1], wb, xf[s][1], wn.b1 = trread(ra + ro + 4096 + 2048))
#undef GAP
    if constexpr (bnd) {
      if constexpr (P2 == 0) {
        constexpr int sg = m2 / 12;
        constexpr int nw1 = m2 < W1POS ? 0 : (m2 - W1POS + 1 > 9 ? 9 : m2 - W1POS + 1);
        static_assert(nis(W1POS - 1) == 54, "pass-1 staging must follow all pass-0 loads");
        waitx<sg * 3, 3, nis(m2) - 9 * (sg + 1) + ((m2 >= TPOS && nis(TPOS) >= 9 * (sg + 1)) ? 1 : 0) + nw1>();
      }
      else if constexpr (h2 == 0) { asm volatile("s_waitcnt vmcnt(%0) lgkmcnt(0)" ::"i"(nst<P, NU>(P == 0 ? 4 * (W1POS + 8 - 36) : 62, 4 * (H - 1))) : "memory"); }
      else { asm volatile("s_waitcnt vmcnt(0) lgkmcnt(0)" ::: "memory"); if constexpr (FFN && NU > 2) publish(c.flag + (P2 - 1) * 128); }
      asm volatile("s_barrier" ::: "memory");
      wn.a0 = trread(ra + ro); wn.a1 = trread(ra + ro + 2048); wn.b0 = trread(ra + ro + 4096); wn.b1 = trread(ra + ro + 4096 + 2048);
      SB();
    }
  }
  template <int P, int NU, int NUP, int NUN, int N0, int N1> DI void steps() {
    if constexpr (N0 < N1) { step<P, NU, NUP, NUN, N0>(); steps<P, NU, NUP, NUN, N0 + 1, N1>(); }
  }
  template <int K> DI void prologue() { if constexpr (K < PRO) { xwload<K>(); prologue<K + 1>(); } }

  template <int NU2> DI void run() {
    prologue<0>();
    steps<0, 4, 0, 4, -2, 72>();
    steps<1, 4, 4, NU2, 0, 72>();
    steps<2, NU2, 4, 0, 0, 18 * NU2>();
    lds_add1(donev);
    MFMA_DRAIN();
    epi_range<2, NU2, 4 * 9 * NU2, epi_start<2, NU2>(NU2 - 1) + EPI_N>();
  }
};

DI unsigned stage_voff(int wave, int lane, unsigned strideB, int nu, int chstride) {
  unsigned ch = ((lane & 7) >> 1) ^ ((lane >> 4) & 3);
  ch = min(ch, (unsigned)(nu - 1));
  return ((wave & 3) * 8 + (lane >> 3)) * strideB + ch * chstride + ((lane & 1) << 4);
}

struct KFArgs {
  const bf16* xnf; const bf16* wqkv; const bf16* wg; const bf16* wu; const bf16* wd; const bf16* wo; const bf16* x;
  bf16* hf; bf16* qkvt; bf16* of; bf16* out; unsigned* cnt; char* sbuf;
};
constexpr int KP_LDS = 163840;

__global__ __launch_bounds__(512, 2) void kp_main(const KFArgs a) {
  extern __shared__ __attribute__((aligned(16))) char lds[];
  const int wave = __builtin_amdgcn_readfirstlane(threadIdx.x >> 6);
  const int w4 = wave & 3;
  const int b = blockIdx.x, xcd = b & 7, j = b >> 3;
  const unsigned ldsbase = (unsigned)(uintptr_t)(LAS char*)lds;
  const int t = xcd * 25 + j;
  const bool valid = j < 25 && t < 198;
  const bool swg = j >= 25 && j < 31;
  const int bmt = valid ? t / 9 : 0, nt = valid ? t % 9 : 0;
  const int mb0 = bmt * 6;
  const int nmb = min(6, 128 - mb0);
  char* cntb = (char*)a.cnt;
  const char* wdb = (const char*)a.wd + nt * 128;
  const unsigned ldsr = ldsbase + wave * 12288;
  if (threadIdx.x < 8) *(volatile LAS unsigned*)(uintptr_t)(ldsbase + KP_LDS - 32 + threadIdx.x * 4) = 0;
  char* lastflag;
  {
    const int lane = fresh_lane();
    const int slot = xcd * 4 + (j & 3), mt = j >> 2;
    const bool ffn = slot < 27;
    const int sq = slot - 27;
    const int us[3] = {ffn ? slot * 8 : sq * 8, ffn ? slot * 8 + 4 : sq * 8 + 4,
                       ffn ? 216 + 8 * (slot / 3) + 3 * (slot % 3) : 40 + 3 * sq};
    const int nu2 = ffn ? (slot % 3 == 2 ? 2 : 3) : (sq == 4 ? 2 : 3);
    const int g = lane >> 4, q = (lane & 15) >> 2, p = lane & 3;
    const unsigned rrow = 4 * g + q;
    const unsigned rdbase = ldsbase + rrow * 128 + (((rrow >> 1) & 3) << 5) + p * 8;
    const int rb = mt * 8 + wave, gmb = rb * 2;
    K1Ctx c;
    c.ldsw = ldsbase + wave * 1024;
    c.xb = (const char*)a.xnf + (size_t)gmb * 18 * 1024;
    c.dopre = __builtin_amdgcn_readfirstlane((valid && wave < 4) ? 1 : 0);
    c.prelds = ldsr;
    c.wdb = wdb; c.wd = (const char*)a.wd; c.wo = (const char*)a.wo; c.w4 = w4; c.tidx = b * 8 + wave; c.tlds = ldsbase + 147456 + wave * 256; c.trow = (mt * 4 + w4) * 18; c.xidx = j * 8 + wave;
    if (ffn) {
      c.strideB = F_ * 2;
      c.flag = cntb + rb * 3 * 128;
      c.tdelta = nu2 * 32 - 4; c.tq = 0;
      const bf16* wsel = wave < 4 ? a.wg : a.wu;
#pragma unroll
      for (int i = 0; i < 3; i++) {
        c.pW[i] = (const char*)(wsel + us[i] * 16);
        c.outp[i] = (char*)a.hf + (size_t)(gmb * 144 + (us[i] >> 1)) * 1024; c.upar[i] = us[i] & 1;
      }
      K1<true> k(c);
      k.rdbase = rdbase; k.xo = lane * 16; k.donev = ldsbase + KP_LDS - 16;
      k.voff4 = stage_voff(wave, lane, c.strideB, 4, 32); k.voff3 = stage_voff(wave, lane, c.strideB, nu2, 32);
      k.sto = (g & 1) * (144 * 1024) + ((g >> 1) * 16 + (lane & 15)) * 16;
      if (nu2 == 3) k.run<3>(); else k.run<2>();
    } else {
      c.strideB = QKVN * 2;
      c.flag = cntb;
      c.tdelta = nu2 * 64 - 64; c.tq = wave < 4 ? 0 : 128;
#pragma unroll
      for (int i = 0; i < 3; i++) {
        c.pW[i] = (const char*)(a.wqkv + us[i] * 32) + (wave < 4 ? 0 : 32);
        c.outp[i] = (char*)a.qkvt + (size_t)(gmb * 54 + us[i]) * 1024; c.upar[i] = 0;
      }
      K1<false> k(c);
      k.rdbase = rdbase; k.xo = lane * 16; k.donev = ldsbase + KP_LDS - 16;
      k.voff4 = stage_voff(wave, lane, c.strideB, 4, 64); k.voff3 = stage_voff(wave, lane, c.strideB, nu2, 64);
      k.sto = lane * 16;
      if (nu2 == 3) k.run<3>(); else k.run<2>();
    }
    lastflag = cntb + (ffn ? rb * 3 + 2 : 192 + rb) * 128;
  }
  const int lane = fresh_lane();
  const int g = lane >> 4, q = (lane & 15) >> 2, p = lane & 3, m = lane & 15;
  const unsigned scol = ((((lane & 7) >> 1) ^ ((lane >> 4) & 1)) << 5) + ((lane & 1) << 4);
  const unsigned voffw0 = (lane >> 3) * (D_ * 2) + scol;
  const unsigned voffw1 = (lane >> 3) * (D_ * 2) + (scol ^ 64);
  const unsigned rrow2 = 8 * g + q;
  const unsigned rdb = ldsr + rrow2 * 128 + ((((rrow2 >> 1) & 1) | ((g & 1) << 1)) << 5) + p * 8;
  auto wstage = [&](const char* wsrc, int sl) {
#pragma unroll
    for (int r = 0; r < 4; r++) glds16(wsrc + r * 8 * (D_ * 2), (r & 1) ? voffw1 : voffw0, ldsr + sl * 4096 + r * 1024);
  };
  auto attention = [&](const int mb, const int hg, const unsigned ldsr, char* ldsp) {
    const int mbp = mb > 0 ? mb - 1 : 0;
    if (wave == 4) {
      pollge(1, cntb + 192 * 128, (((lane & 1) ? mbp : mb) >> 1) * 128, 5u);
      lds_add1(ldsbase + KP_LDS - 32);
    } else {
      lds_wait_slow<0>(ldsbase + KP_LDS - 32, 1);
    }
    const int c0 = 72 * hg;
    const int v0 = c0 >> 5;
#pragma unroll
    for (int sec = 0; sec < 5; sec++) {
      const int smb = (sec == 1 || sec == 3) ? mbp : mb;
      const int ub = (sec == 0 ? 0 : (sec <= 2 ? 18 : 36));
#pragma unroll
      for (int v = 0; v < 3; v++)
        glds16_sc1((const char*)a.qkvt + (size_t)(smb * 54 + ub + v0 + v) * 1024, lane * 16, ldsr + (sec * 3 + v) * 1024);
    }
    const int odd = g & 1;
    const int cb = c0 - v0 * 32 + g * DH - 2 * odd;
    int coff[5];
#pragma unroll
    for (int i = 0; i < 5; i++) {
      const int cidx = cb + 4 * i, cc = cidx & 31;
      coff[i] = (cidx >> 5) * 1024 + ((cc >> 2) & 3) * 256 + (cc >> 4) * 8;
    }
    asm volatile("s_waitcnt vmcnt(0)" ::: "memory");
    u32x2 qv[5];
#pragma unroll
    for (int i = 0; i < 5; i++) qv[i] = *(const u32x2*)(ldsp + coff[i] + m * 16);
    qv[0][0] = odd ? 0u : qv[0][0];
    qv[4][1] = odd ? qv[4][1] : 0u;
    const int srow = mb * 16 + m;
    float lg[8]; float mx = -3.0e38f;
#pragma unroll
    for (int w = 0; w < 8; w++) {
      const int r = m + 16 - 2 * w;
      const char* kp = ldsp + 3072 + (r >> 4) * 3072 + (r & 15) * 16;
      float s_ = 0;
#pragma unroll
      for (int i = 0; i < 5; i++) {
        u32x2 kv = *(const u32x2*)(kp + coff[i]);
        if (i == 0) kv[0] = odd ? 0u : kv[0];
        if (i == 4) kv[1] = odd ? kv[1] : 0u;
        s_ = dot2(qv[i][0], kv[0], s_); s_ = dot2(qv[i][1], kv[1], s_);
      }
      s_ = rbf(rbf(s_) * 0.23570226039551584f);
      s_ = (srow - 2 * w) >= 0 ? s_ : -1e9f;
      lg[w] = s_; mx = fmaxf(mx, s_);
      if ((w & 3) == 3) SB();
    }
    float sum = 0;
#pragma unroll
    for (int w = 0; w < 8; w++) { lg[w] = __expf(lg[w] - mx); sum += lg[w]; }
    const float inv = 1.f / sum;
    float o[20];
#pragma unroll
    for (int i = 0; i < 20; i++) o[i] = 0;
#pragma unroll
    for (int w = 0; w < 8; w += 2) {
      const int r0 = m + 16 - 2 * w, r1 = r0 - 2;
      const char* vp0 = ldsp + 9216 + (r0 >> 4) * 3072 + (r0 & 15) * 16;
      const char* vp1 = ldsp + 9216 + (r1 >> 4) * 3072 + (r1 & 15) * 16;
      const unsigned pw = pk2(lg[w] * inv, lg[w + 1] * inv);
#pragma unroll
      for (int i = 0; i < 5; i++) {
        const u32x2 u0 = *(const u32x2*)(vp0 + coff[i]), u1 = *(const u32x2*)(vp1 + coff[i]);
#pragma unroll
        for (int h = 0; h < 2; h++) {
          o[4 * i + 2 * h] = dot2(__builtin_amdgcn_perm(u1[h], u0[h], 0x05040100u), pw, o[4 * i + 2 * h]);
          o[4 * i + 2 * h + 1] = dot2(__builtin_amdgcn_perm(u1[h], u0[h], 0x07060302u), pw, o[4 * i + 2 * h + 1]);
        }
      }
      if (w & 2) SB();
    }
#pragma unroll
    for (int i = 0; i < 10; i++) {
      const int cl = g * DH - 2 * odd + 2 * i;
      const bool ok = odd ? (i > 0) : (i < 9);
      const int ad = ok ? ((cl >> 3) * 16 + m) * 16 + (cl & 7) * 2 : 2304 + lane * 4;
      *(unsigned*)(ldsp + ad) = pk2(o[2 * i], o[2 * i + 1]);
    }
    asm volatile("s_waitcnt lgkmcnt(0)" ::: "memory");
    {
      char* ob = (char*)a.of + (size_t)mb * 18 * 1024 + (c0 >> 3) * 256;
      u32x4 v0_ = *(const u32x4*)(ldsp + lane * 16), v1_ = *(const u32x4*)(ldsp + 1024 + lane * 16), v2_ = *(const u32x4*)(ldsp + 2048 + (lane & 15) * 16);
      gst16_sc1(ob, lane * 16, v0_);
      gst16_sc1(ob + 1024, lane * 16, v1_);
      gst16_sc1_m(ob + 2048, lane * 16, v2_, 0xffffull);
    }
    asm volatile("s_waitcnt vmcnt(0) lgkmcnt(0)" ::: "memory");
    publish(cntb + (256 + mb) * 128);
  };
  auto helper_attention = [&]() {
    __builtin_amdgcn_s_setprio(2);
    lds_wait_slow<0>(ldsbase + KP_LDS - 16, 8);
    const int sb = (swg ? 98304 : 49152) + w4 * 15360;
    attention(xcd * 16 + (j >> 1), (j & 1) * 4 + w4, ldsbase + sb, lds + sb);
    lds_add1(ldsbase + KP_LDS - 12);
    __builtin_amdgcn_s_setprio(0);
  };
  if (wave >= 4 || swg) {
    asm volatile("s_waitcnt vmcnt(0)" ::: "memory");
    publish(lastflag);
  }
  if (wave >= 4) helper_attention();
  if (swg) {
    const int si = j - 25;
    const int mb = xcd * 16 + (si / 3) * 8 + wave, seg0 = (si % 3) * 3;
    const char* wsrc = (const char*)a.wo + seg0 * 128;
    auto stage_piece = [&](int grp, int pc, unsigned slot) {
      const int ks = pc / 12, img = (pc % 12) >> 2, r = pc & 3;
      glds16(wsrc + (size_t)((grp * 6 + ks) * 32 + r * 8) * (D_ * 2) + img * 128, (r & 1) ? voffw1 : voffw0, ldsbase + slot + pc * 1024);
    };
    if (wave < 4) {
#pragma unroll 2
      for (int i = 0; i < 18; i++) stage_piece(0, i * 4 + wave, 0);
    }
    __syncthreads();
#pragma unroll 3
    for (int i = 0; i < 9; i++) stage_piece(1, i * 8 + wave, 73728);
    pollge_slow(cntb + (256 + mb) * 128, 0u, 8u);
    bf16x8 ob[18];
#pragma unroll
    for (int k = 0; k < 18; k++) gld16_sc1(ob[k], (const char*)a.of + (size_t)(mb * 18 + k) * 1024, lane * 16);
    f32x4 sa[12];
#pragma unroll
    for (int c = 0; c < 12; c++) { sa[c] = f32x4{0, 0, 0, 0}; asm volatile("" : "+v"(sa[c])); }
    const unsigned rd0 = rdb - ldsr + ldsbase;
#pragma unroll
    for (int k = 0; k < 18; k += 6)
      asm volatile("s_waitcnt vmcnt(0)" : "+v"(ob[k]), "+v"(ob[k + 1]), "+v"(ob[k + 2]), "+v"(ob[k + 3]), "+v"(ob[k + 4]), "+v"(ob[k + 5]) :: "memory");
#pragma unroll
    for (int grp = 0; grp < 3; grp++) {
      asm volatile("s_waitcnt vmcnt(0) lgkmcnt(0)\n\ts_barrier" ::: "memory");
      if (grp == 1) {
#pragma unroll 3
        for (int i = 0; i < 9; i++) stage_piece(2, i * 8 + wave, 0);
      }
      const unsigned sl = rd0 + (grp & 1) * 73728;
      s16x4 a0[2][4], a1[2][4];
      unsigned adc[4];
#pragma unroll
      for (int c = 0; c < 4; c++) { adc[c] = sl ^ (c << 5); a0[0][c] = trread(adc[c]); a1[0][c] = trread(adc[c] + 512); }
#pragma unroll
      for (int n = 0; n < 18; n++) {
        if (n == 9) {
#pragma unroll
          for (int c = 0; c < 4; c++) { adc[c] += 36864; asm volatile("" : "+v"(adc[c])); }
        }
#pragma unroll
        for (int c = 0; c < 4; c++) {
          bf16x8 wa = cat8(a0[n & 1][c], a1[n & 1][c]);
          MFMA(sa[(n % 3) * 4 + c], wa, ob[grp * 6 + n / 3]);
          SB();
          if (n < 17) {
            const unsigned o_ = (n + 1 - (n >= 9 ? 9 : 0)) * 4096;
            a0[(n + 1) & 1][c] = trread(adc[c] + o_); a1[(n + 1) & 1][c] = trread(adc[c] + o_ + 512);
          }
          SB();
        }
      }
    }
    MFMA_DRAIN();
#pragma unroll
    for (int cp = 0; cp < 6; cp++) {
      const f32x4 u = sa[2 * cp], v = sa[2 * cp + 1];
      u32x4 o = {pk2(u[0], u[1]), pk2(u[2], u[3]), pk2(v[0], v[1]), pk2(v[2], v[3])};
      gst16_sc1(a.sbuf + (size_t)((mb * 9 + seg0) * 2 + cp) * 1024, lane * 16, o);
    }
    asm volatile("s_waitcnt vmcnt(0)" ::: "memory");
    publish(cntb + (384 + mb) * 128);
    return;
  }
  const int i0 = (wave >> 2) * 3;
  const int col = nt * 64 + w4 * 16 + 4 * g;
  u32x2 xv[3];
  unsigned fl0;
  const unsigned voffc = ((mb0 + min(lane, nmb - 1)) >> 1) * (3 * 128);
  {
#pragma unroll
    for (int ii = 0; ii < 3; ii++) {
      const unsigned xo = (unsigned)(min((mb0 + i0 + ii) * 16 + m, S_ - 1) * D_ + col) * 2u;
      asm volatile("global_load_dwordx2 %0, %1, %2" : "=v"(xv[ii]) : "v"(xo), "s"(a.x) : "memory");
    }
    asm volatile("global_load_dword %0, %1, %2 offset:128 sc1" : "=v"(fl0) : "v"(voffc), "s"(cntb) : "memory");
    asm volatile("s_waitcnt vmcnt(0)" : "+v"(xv[0]), "+v"(xv[1]), "+v"(xv[2]), "+v"(fl0) :: "memory");
    if (wave < 4) publish(lastflag);
  }
  if (!valid) return;
  u32x2 sv[3];
  if (wave < 4) {
    f32x4 acc[6][4];
    bf16x8 hb[3][6];
    unsigned voffh[6];
#pragma unroll
    for (int i = 0; i < 6; i++) voffh[i] = (mb0 + min(i, nmb - 1)) * 144 * 1024 + lane * 16;
    {
      int need;
      const int rv = __all(fl0 >= 27u) ? 0 : 1;
      asm volatile("s_nop 0\n\tv_readfirstlane_b32 %0, %1" : "=s"(need) : "v"(rv));
      pollge(need, cntb + 128, voffc, 27u);
    }
    const char* hsrc = (const char*)a.hf + w4 * 1024;
    const char* wsrc = wdb + (size_t)(12 + w4) * 32 * (D_ * 2);
    auto compute = [&](bf16x8 (&src)[6], int sl) {
      s16x4 a0[4], a1[4];
#pragma unroll
      for (int c = 0; c < 4; c++) { unsigned ad = (rdb ^ (c << 5)) + sl * 4096; a0[c] = trread(ad); a1[c] = trread(ad + 512); }
      SB();
#pragma unroll
      for (int c = 0; c < 4; c++) {
        bf16x8 wa = cat8(a0[c], a1[c]);
#pragma unroll
        for (int i = 0; i < 6; i++) MFMA(acc[i][c], wa, src[i]);
      }
    };
#define WAITH(n, d) asm volatile("s_waitcnt vmcnt(%6)" : "+v"(hb[d][0]), "+v"(hb[d][1]), "+v"(hb[d][2]), "+v"(hb[d][3]), "+v"(hb[d][4]), "+v"(hb[d][5]) : "i"(n) : "memory")
    auto hload = [&](bf16x8 (&dst)[6]) {
#pragma unroll
      for (int i = 0; i < 6; i++) gld16_sc1(dst[i], hsrc, voffh[i]);
      hsrc += 4096;
    };
    auto body = [&](bf16x8 (&src)[6], int sl) {
      compute(src, sl);
      wstage(wsrc, sl);
      wsrc += 4 * 32 * (D_ * 2);
      hload(src);
    };
    hload(hb[0]); hload(hb[1]); hload(hb[2]);
#pragma unroll
    for (int i = 0; i < 6; i++)
#pragma unroll
      for (int c = 0; c < 4; c++) { acc[i][c] = f32x4{0, 0, 0, 0}; asm volatile("" : "+v"(acc[i][c])); }
    WAITH(12, 0); body(hb[0], 0);
    WAITH(16, 1); body(hb[1], 1);
    WAITH(20, 2); body(hb[2], 2);
#pragma unroll 1
    for (int it = 3; it < 33; it += 3) {
      lds_wait_if<4>((unsigned)(it ^ 24), ldsbase + KP_LDS - 32, 1u);
      WAITH(20, 0); body(hb[0], 0);
      WAITH(20, 1); body(hb[1], 1);
      WAITH(20, 2); body(hb[2], 2);
    }
    lds_wait<4>(ldsbase + KP_LDS - 16, 4);
    WAITH(20, 0); compute(hb[0], 0);
    WAITH(10, 1); compute(hb[1], 1);
    WAITH(0, 2); compute(hb[2], 2);
    MFMA_DRAIN();
#pragma unroll
    for (int i = 0; i < 6; i++)
#pragma unroll
      for (int c = 0; c < 4; c++) *(f32x4*)(lds + 49152 + wave * 24576 + ((c * 6 + i) * 64 + lane) * 16) = acc[i][c];
  } else {
    if (wave == 4) {
      pollge(1, cntb + 2 * 128, voffc, 27u);
      lds_add1(ldsbase + KP_LDS - 28);
      __builtin_amdgcn_s_sleep(127);
      pollge_slow(cntb + 384 * 128, (mb0 + min(lane, nmb - 1)) * 128, 3u);
      lds_add1(ldsbase + KP_LDS - 24);
    } else {
      lds_wait_slow<8>(ldsbase + KP_LDS - 32, 1);
    }
    u32x2 sp[3];
#pragma unroll
    for (int ii = 0; ii < 6; ii++) {
      const unsigned so = (unsigned)(((mb0 + min(ii, nmb - 1)) * 9 + nt) * 2 + (w4 >> 1)) * 1024u + lane * 16 + (w4 & 1) * 8;
      if (ii < 3) asm volatile("global_load_dwordx2 %0, %1, %2 sc1" : "=v"(sp[ii]) : "v"(so), "s"(a.sbuf) : "memory");
      else asm volatile("global_load_dwordx2 %0, %1, %2 sc1" : "=v"(sv[ii - 3]) : "v"(so), "s"(a.sbuf) : "memory");
    }
    asm volatile("s_waitcnt vmcnt(0)" : "+v"(sp[0]), "+v"(sp[1]), "+v"(sp[2]), "+v"(sv[0]), "+v"(sv[1]), "+v"(sv[2]) :: "memory");
#pragma unroll
    for (int ii = 0; ii < 3; ii++) *(u32x2*)(lds + 147456 + ((w4 * 3 + ii) * 64 + lane) * 8) = sp[ii];
  }
  __syncthreads();
  if (wave < 4) {
#pragma unroll
    for (int ii = 0; ii < 3; ii++) sv[ii] = *(const u32x2*)(lds + 147456 + ((w4 * 3 + ii) * 64 + lane) * 8);
  }
#pragma unroll
  for (int ii = 0; ii < 3; ii++) {
    const int i = i0 + ii;
    if (i < nmb) {
      const char* pp = lds + 49152 + ((w4 * 6 + i) * 64 + lane) * 16;
      f32x4 d4 = (*(const f32x4*)(pp) + *(const f32x4*)(pp + 24576)) + (*(const f32x4*)(pp + 2 * 24576) + *(const f32x4*)(pp + 3 * 24576));
      u32x2 r;
#pragma unroll
      for (int h = 0; h < 2; h++) {
        const unsigned db = pk2(d4[2 * h], d4[2 * h + 1]);
        const float d0 = lo2f(db), d1 = hi2f(db);
        const float g0 = d0 * __builtin_amdgcn_rcpf(1.f + __builtin_amdgcn_exp2f(d0 * -1.4426950408889634f));
        const float g1 = d1 * __builtin_amdgcn_rcpf(1.f + __builtin_amdgcn_exp2f(d1 * -1.4426950408889634f));
        const unsigned gb = pk2(g0, g1);
        const unsigned pb = pk2(lo2f(sv[ii][h]) * lo2f(gb), hi2f(sv[ii][h]) * hi2f(gb));
        r[h] = pk2(lo2f(xv[ii][h]) + lo2f(pb), hi2f(xv[ii][h]) + hi2f(pb));
      }
      const int row = (mb0 + i) * 16 + m;
      *(u32x2*)((char*)a.out + ((size_t)row * D_ + col) * 2) = r;
    }
  }
}

extern "C" void kernel_launch(void* const* d_in, const int* in_sizes, int n_in, void* d_out, int out_size, void* d_ws,
                              size_t ws_size, hipStream_t stream) {
  KFArgs a;
  a.x = (const bf16*)d_in[0];
  a.wqkv = (const bf16*)d_in[1];
  a.wo = (const bf16*)d_in[2];
  a.wg = (const bf16*)d_in[4];
  a.wu = (const bf16*)d_in[5];
  a.wd = (const bf16*)d_in[6];
  a.out = (bf16*)d_out;
  char* ws = (char*)d_ws;
  bf16* xnf = (bf16*)ws; ws += (size_t)S_ * D_ * 2;
  a.xnf = xnf;
  a.of = (bf16*)ws; ws += (size_t)S_ * D_ * 2;
  a.qkvt = (bf16*)ws; ws += (size_t)128 * 54 * 1024;
  a.hf = (bf16*)ws; ws += (size_t)S_ * F_ * 2;
  a.cnt = (unsigned*)ws; ws += 512 * 128;
  a.sbuf = ws; ws += (size_t)S_ * D_ * 2;
  (void)hipFuncSetAttribute((const void*)kp_main, hipFuncAttributeMaxDynamicSharedMemorySize, KP_LDS);
  k0_ln<<<256, 256, 0, stream>>>(a.x, xnf, a.cnt, a.wg, a.wu, a.wqkv);
  kp_main<<<256, 512, KP_LDS, stream>>>(a);
}
```
